```python
import torch

def _softmax(x, axis=-1):
    return torch.softmax(x, dim=axis)
def _gelu(x):
    return torch.nn.functional.gelu(x, approximate="tanh")
def _silu(x):
    return torch.nn.functional.silu(x)
def _sigmoid(x):
    return torch.sigmoid(x)
def _layernorm(x, axis=-1, eps=1e-5):
    m = x.mean(dim=axis, keepdim=True)
    v = x.var(dim=axis, keepdim=True, unbiased=False)
    return (x - m) / torch.sqrt(v + eps)
def _scatter_add(tgt, idx, src, axis=0):
    return tgt.clone().index_add_(axis, idx, src)
def _segment_sum(src, seg, n):
    out = torch.zeros((n,) + src.shape[1:], dtype=src.dtype)
    return out.index_add_(0, seg, src)
def _pad(x, pw):
    flat = tuple(v for pair in reversed(pw) for v in pair)
    return torch.nn.functional.pad(x, flat)

kernel_name = 'gated_mix__grouped_kv_mqa__mixer_token_mlp__prompt'

def setup_inputs():

    _w = (lambda a: (lambda t: t.to(torch.bfloat16) if t.is_floating_point() else t)(torch.as_tensor(a)))
    def _A_setup():
        B, S, D, H = 1, 2048, 640, 32
        Dh = D // H
        return {
            "x": _w((lambda *s: torch.randn(*s, dtype=torch.bfloat16) * 0.05)(B, S, D)),
            "wq": _w((lambda *s: torch.randn(*s, dtype=torch.bfloat16) * 0.8)(D, H * Dh)),
            "wk": _w((lambda *s: torch.randn(*s, dtype=torch.bfloat16) * 0.8)(D, Dh)),
            "wv": _w((lambda *s: torch.randn(*s, dtype=torch.bfloat16) * 0.05)(D, Dh)),
            "wo": _w((lambda *s: torch.randn(*s, dtype=torch.bfloat16) * 0.05)(H * Dh, D)),
        }
    _A_in = _A_setup()

    def _B_setup():
        B, S, D, F = 1, 2048, 640, 1536
        return {
            "x": _w((lambda *s: torch.randn(*s, dtype=torch.bfloat16) * 0.05)(B, S, D)),
            "w1": _w((lambda *s: torch.randn(*s, dtype=torch.bfloat16) * 0.2)(S, F) * (S ** -0.5)),
            "b1": _w((lambda *s: torch.randn(*s, dtype=torch.bfloat16) * 3.2)(F) * 0.02),
            "w2": _w((lambda *s: torch.randn(*s, dtype=torch.bfloat16) * 0.05)(F, S) * (F ** -0.5)),
            "b2": _w((lambda *s: torch.randn(*s, dtype=torch.bfloat16) * 0.1)(S) * 0.02),
        }
    _B_in = _B_setup()

    return {
        "A_x": _A_in["x"],
        "A_wq": _A_in["wq"],
        "A_wk": _A_in["wk"],
        "A_wv": _A_in["wv"],
        "A_wo": _A_in["wo"],
        "B_x": _B_in["x"],
        "B_w1": _B_in["w1"],
        "B_b1": _B_in["b1"],
        "B_w2": _B_in["w2"],
        "B_b2": _B_in["b2"],
        "gate_w": _w((lambda *s: torch.randn(*s, dtype=torch.bfloat16) * 0.05)(640) * 16.0),
    }

def reference(A_x, A_wq, A_wk, A_wv, A_wo, B_x, B_w1, B_b1, B_w2, B_b2, gate_w):
    def _A_ref(x, wq, wk, wv, wo):
        B, S, D = x.shape
        H = 32
        Dh = D // H
        q = (lambda x, *d: x.permute(*d))((x @ wq).reshape(B, S, H, Dh), 0, 2, 1, 3)
        k = x @ wk
        v = x @ wv
        scores = torch.einsum("bhqd,bkd->bhqk", q, k) * (Dh ** -0.5)
        p = _softmax(scores, axis=-1)
        o = torch.einsum("bhqk,bkd->bhqd", p, v)
        return (lambda x, *d: x.permute(*d))(o, 0, 2, 1, 3).reshape(B, S, H * Dh) @ wo

    def _B_ref(x, w1, b1, w2, b2):
        B, S, D = x.shape
        h = _layernorm(x, axis=-1)
        u = torch.einsum("bsd,sf->bdf", h, w1) + b1
        u = _gelu(u)
        y = torch.einsum("bdf,fs->bds", u, w2) + b2
        return (lambda x, *d: x.permute(*d))(y, 0, 2, 1)

    x = A_x
    g = _sigmoid((x * gate_w).sum(-1, keepdims=True))
    a = _A_ref(x, A_wq, A_wk, A_wv, A_wo)
    b = _B_ref(x, B_w1, B_b1, B_w2, B_b2)
    return g * a + (1 - g) * b
```

```cpp
#include <hip/hip_runtime.h>
#include <hip/hip_bf16.h>
#include <stdint.h>

typedef unsigned short u16;
typedef __attribute__((ext_vector_type(8))) short bf16x8;
typedef __attribute__((ext_vector_type(4))) short s16x4;
typedef __attribute__((ext_vector_type(16))) float f32x16;
typedef __attribute__((ext_vector_type(4))) unsigned u32x4;
typedef __attribute__((ext_vector_type(2))) unsigned u32x2;
typedef __attribute__((ext_vector_type(2))) __bf16 bf16x2_t;
#define LDS_AS __attribute__((address_space(3)))
#define DI __device__ __forceinline__

constexpr int S_ = 2048, D_ = 640, F_ = 1536;

constexpr size_t WS_Q   = 0;
constexpr size_t WS_H   = WS_Q + (size_t)S_ * D_ * 2;
constexpr size_t WS_O   = WS_H + (size_t)S_ * D_ * 2;
constexpr size_t WS_U   = WS_O + (size_t)S_ * D_ * 2;
constexpr size_t WS_KV  = WS_U + (size_t)D_ * F_ * 2;
constexpr int    KV_CHUNK = 21504;
constexpr int    KV_KB_OFF = 8192, KV_V_OFF = 10240;
constexpr size_t WS_KN  = WS_KV + 8 * KV_CHUNK;
constexpr size_t WS_G   = WS_KN + S_ * 4;

DI void glds16(const void* g, void* lds) {
  __builtin_amdgcn_global_load_lds((const unsigned*)g, (unsigned*)lds, 16, 0, 0);
}
DI unsigned pk_bf16(float a, float b) {
  bf16x2_t p; p[0] = (__bf16)a; p[1] = (__bf16)b; return __builtin_bit_cast(unsigned, p);
}
DI float bf_lo(unsigned u) { return __uint_as_float(u << 16); }
DI float bf_hi(unsigned u) { return __uint_as_float(u & 0xffff0000u); }
DI float bf2f(u16 v) { return __uint_as_float((unsigned)v << 16); }
DI u16 f2bf(float f) { return (u16)(pk_bf16(f, 0.f) & 0xffffu); }
DI f32x16 mfma32(bf16x8 a, bf16x8 b, f32x16 c) { return __builtin_amdgcn_mfma_f32_32x32x16_bf16(a, b, c, 0, 0, 0); }
DI f32x16 mfma32k8(s16x4 a, s16x4 b, f32x16 c) { return __builtin_amdgcn_mfma_f32_32x32x8bf16_1k(a, b, c, 0, 0, 0); }
DI s16x4 tr_read(const char* p) {
  return __builtin_amdgcn_ds_read_tr16_b64_v4i16((LDS_AS s16x4*)p);
}
#define WAIT_VM(n) asm volatile("s_waitcnt vmcnt(" #n ")" ::: "memory")
template <int N> DI void wait_vm() {
  if constexpr (N == 0) WAIT_VM(0);
  else if constexpr (N == 2) WAIT_VM(2);
  else if constexpr (N == 3) WAIT_VM(3);
  else if constexpr (N == 4) WAIT_VM(4);
  else if constexpr (N == 6) WAIT_VM(6);
  else if constexpr (N == 8) WAIT_VM(8);
  else if constexpr (N == 9) WAIT_VM(9);
  else if constexpr (N == 12) WAIT_VM(12);
  else if constexpr (N == 16) WAIT_VM(16);
  else static_assert(N < 0, "add case");
}
DI void raw_barrier() {
  asm volatile("s_waitcnt lgkmcnt(0)" ::: "memory");
  __builtin_amdgcn_s_barrier();
}

template <int R, int NT> DI void stage_kc(char* lds, const u16* g, int ld, int tid) {
  constexpr int N = R * 8 / NT;
#pragma unroll
  for (int i = 0; i < N; ++i) {
    const int idx = i * NT + tid, row = idx >> 3, ch = (idx & 7) ^ ((row >> 1) & 7);
    glds16(g + (size_t)row * ld + ch * 8, lds + idx * 16);
  }
}
template <int C> DI int swz_ks(int k) { return C == 64 ? (((k >> 1) & 1) << 2) : ((k & 3) << 2); }
template <int C, int NT> DI void stage_ks(char* lds, const u16* g, int ld, int tid) {
  constexpr int CPR = C / 8, N = 64 * CPR / NT;
#pragma unroll
  for (int i = 0; i < N; ++i) {
    const int idx = i * NT + tid, k = idx / CPR, ch = (idx % CPR) ^ swz_ks<C>(k);
    glds16(g + (size_t)k * ld + ch * 8, lds + idx * 16);
  }
}
DI bf16x8 frag_kc(const char* lds, int row0, int ks, int lane) {
  const int row = row0 + (lane & 31), ch = (2 * ks + (lane >> 5)) ^ ((row >> 1) & 7);
  return *(const bf16x8*)(lds + row * 128 + ch * 16);
}
template <int C> DI bf16x8 frag_ks(const char* lds, int c0, int ks, int lane) {
  const int h = lane >> 5, blk = (lane >> 4) & 1, q = (lane & 15) >> 2, p = lane & 3;
  const int k = 16 * ks + 8 * h + q, ch = ((c0 >> 3) + 2 * blk + (p >> 1)) ^ swz_ks<C>(q);
  const char* a = lds + k * (2 * C) + ch * 16 + 8 * (p & 1);
  const s16x4 lo = tr_read(a), hi = tr_read(a + 4 * (2 * C));
  return __builtin_shufflevector(lo, hi, 0, 1, 2, 3, 4, 5, 6, 7);
}

template <int BM, int BN, int WGM, int WGN, bool AKC, bool BKC, int NBUF>
DI void gemm_loop(f32x16 (&acc)[BM / WGM / 32][BN / WGN / 32], char* lds, const u16* A, int lda,
                  const u16* B, int ldb, int nk, int tid) {
  constexpr int NT = WGM * WGN * 64, TM = BM / WGM / 32, TN = BN / WGN / 32;
  constexpr int AB = BM * 128, BB = BN * 128, STG = AB + BB, G = STG / (NT * 16);
  const int lane = tid & 63, wid = tid >> 6, wm = wid / WGN, wn = wid % WGN;
  auto stage = [&](int kt, int buf) {
    char* l = lds + buf * STG;
    if constexpr (AKC) stage_kc<BM, NT>(l, A + kt * 64, lda, tid);
    else stage_ks<BM, NT>(l, A + (size_t)kt * 64 * lda, lda, tid);
    if constexpr (BKC) stage_kc<BN, NT>(l + AB, B + kt * 64, ldb, tid);
    else stage_ks<BN, NT>(l + AB, B + (size_t)kt * 64 * ldb, ldb, tid);
  };
#pragma unroll
  for (int i = 0; i < NBUF - 1; ++i) stage(i < nk ? i : nk - 1, i);
  int buf = 0;
  for (int kt = 0; kt < nk; ++kt) {
    wait_vm<G * (NBUF - 2)>();
    raw_barrier();
    {
      const int kn = kt + NBUF - 1;
      int bn = buf + NBUF - 1; if (bn >= NBUF) bn -= NBUF;
      stage(kn < nk ? kn : nk - 1, bn);
    }
    const char* la = lds + buf * STG;
    const char* lb = la + AB;
#pragma unroll
    for (int ks = 0; ks < 4; ++ks) {
      bf16x8 af[TM], bfr[TN];
#pragma unroll
      for (int i = 0; i < TM; ++i) {
        if constexpr (AKC) af[i] = frag_kc(la, wm * (BM / WGM) + i * 32, ks, lane);
        else af[i] = frag_ks<BM>(la, wm * (BM / WGM) + i * 32, ks, lane);
      }
#pragma unroll
      for (int j = 0; j < TN; ++j) {
        if constexpr (BKC) bfr[j] = frag_kc(lb, wn * (BN / WGN) + j * 32, ks, lane);
        else bfr[j] = frag_ks<BN>(lb, wn * (BN / WGN) + j * 32, ks, lane);
      }
#pragma unroll
      for (int i = 0; i < TM; ++i)
#pragma unroll
        for (int j = 0; j < TN; ++j) acc[i][j] = mfma32(af[i], bfr[j], acc[i][j]);
    }
    ++buf; if (buf == NBUF) buf = 0;
  }
  wait_vm<0>();
  raw_barrier();
}

DI int crow(int r, int h) { return (r & 3) + 8 * (r >> 2) + 4 * h; }

constexpr int L1_NQ = 32 * 10, L1_NKV = 32, L1_NLN = 64;
constexpr int L1_LDS = 64 * 1024;
constexpr float QSCALE = 0.22360679774997896f * 1.4426950408889634f;

__global__ __launch_bounds__(256) void k_proj(const u16* __restrict__ x, const u16* __restrict__ wq,
                                              const u16* __restrict__ wk, const u16* __restrict__ wv,
                                              const u16* __restrict__ gw, char* __restrict__ ws) {
  extern __shared__ __attribute__((aligned(16))) char lds[];
  const int tid = threadIdx.x, lane = tid & 63, wid = tid >> 6;
  const int bid = blockIdx.x;
  if (bid < L1_NQ) {
    const int mt = bid / 10, nt = bid % 10;
    f32x16 acc[1][1] = {};
    gemm_loop<64, 64, 2, 2, true, false, 4>(acc, lds, x + (size_t)mt * 64 * D_, D_, wq + nt * 64, D_, 10, tid);
    u16* q = (u16*)(ws + WS_Q);
    const int wm = wid >> 1, wn = wid & 1, h = lane >> 5;
    const int col = nt * 64 + wn * 32 + (lane & 31);
#pragma unroll
    for (int r = 0; r < 16; ++r) {
      const int row = mt * 64 + wm * 32 + crow(r, h);
      q[(size_t)row * D_ + col] = f2bf(acc[0][0][r] * QSCALE);
    }
  } else if (bid < L1_NQ + L1_NKV) {
    const int mt = bid - L1_NQ;
    char* lwk = lds; char* lwv = lds + 25600 + 256; char* lx = lds + 2 * (25600 + 256);
    for (int i = tid; i < 1600; i += 256) {
      glds16(wk + i * 8, lwk + i * 16);
      glds16(wv + i * 8, lwv + i * 16);
    }
    const u16* xa = x + (size_t)mt * 64 * D_;
    const int wm = wid >> 1, isv = wid & 1, h = lane >> 5;
    const int blk = (lane >> 4) & 1, qq = (lane & 15) >> 2, p = lane & 3;
    f32x16 acc = {};
    stage_kc<64, 256>(lx, xa, D_, tid);
    for (int kt = 0; kt < 10; ++kt) {
      wait_vm<0>();
      raw_barrier();
      if (kt + 1 < 10) stage_kc<64, 256>(lx + ((kt + 1) & 1) * 8192, xa + (kt + 1) * 64, D_, tid);
      const char* la = lx + (kt & 1) * 8192;
      const char* lw = isv ? lwv : lwk;
#pragma unroll
      for (int ks = 0; ks < 4; ++ks) {
        const bf16x8 xf = frag_kc(la, wm * 32, ks, lane);
        const char* a = lw + (kt * 64 + 16 * ks + 8 * h + qq) * 40 + (16 * blk + 4 * p) * 2;
        const s16x4 lo = tr_read(a), hi = tr_read(a + 4 * 40);
        const bf16x8 wf = __builtin_shufflevector(lo, hi, 0, 1, 2, 3, 4, 5, 6, 7);
        if (isv) acc = mfma32(xf, wf, acc);
        else     acc = mfma32(wf, xf, acc);
      }
    }
    const int tile = mt * 2 + wm;
    char* img = ws + WS_KV + (size_t)(tile >> 3) * KV_CHUNK;
    const int tl = tile & 7;
    if (!isv) {
      u32x4 a; a[0] = pk_bf16(acc[0], acc[1]); a[1] = pk_bf16(acc[2], acc[3]);
      a[2] = pk_bf16(acc[4], acc[5]); a[3] = pk_bf16(acc[6], acc[7]);
      *(u32x4*)(img + tl * 1024 + lane * 16) = a;
      float n2 = 0.f;
#pragma unroll
      for (int i = 0; i < 4; ++i) { float lo = bf_lo(a[i]), hi = bf_hi(a[i]); n2 += lo * lo + hi * hi; }
      if (h == 0) {
        u32x2 b; b[0] = pk_bf16(acc[8], acc[9]); b[1] = pk_bf16(acc[10], acc[11]);
        *(u32x2*)(img + KV_KB_OFF + tl * 256 + lane * 8) = b;
#pragma unroll
        for (int i = 0; i < 2; ++i) { float lo = bf_lo(b[i]), hi = bf_hi(b[i]); n2 += lo * lo + hi * hi; }
      }
      n2 += __shfl_xor(n2, 32);
      if (h == 0) ((float*)(ws + WS_KN))[tile * 32 + lane] = n2;
    } else {
      const int d = lane & 31;
      if (d <= 20) {
#pragma unroll
        for (int s = 0; s < 2; ++s) {
          u32x4 a;
#pragma unroll
          for (int i = 0; i < 4; ++i) a[i] = d < 20 ? pk_bf16(acc[8 * s + 2 * i], acc[8 * s + 2 * i + 1]) : 0x3f803f80u;
          *(u32x4*)(img + KV_V_OFF + tl * 1344 + (s * 2 + h) * 336 + d * 16) = a;
        }
      }
    }
  } else {
    const int b = bid - L1_NQ - L1_NKV;
    unsigned gwv[5];
#pragma unroll
    for (int i = 0; i < 5; ++i) gwv[i] = ((const unsigned*)gw)[lane + 64 * i];
    for (int t = 0; t < 8; ++t) {
      const int s = b * 32 + wid * 8 + t;
      const unsigned* xr = (const unsigned*)(x + (size_t)s * D_);
      unsigned xv[5];
#pragma unroll
      for (int i = 0; i < 5; ++i) xv[i] = xr[lane + 64 * i];
      float sum = 0.f, gs = 0.f;
#pragma unroll
      for (int i = 0; i < 5; ++i) {
        sum += bf_lo(xv[i]) + bf_hi(xv[i]);
        gs += bf_lo(xv[i]) * bf_lo(gwv[i]) + bf_hi(xv[i]) * bf_hi(gwv[i]);
      }
#pragma unroll
      for (int o = 32; o > 0; o >>= 1) { sum += __shfl_xor(sum, o); gs += __shfl_xor(gs, o); }
      const float mean = sum * (1.f / D_);
      float vs = 0.f;
#pragma unroll
      for (int i = 0; i < 5; ++i) {
        const float a = bf_lo(xv[i]) - mean, c = bf_hi(xv[i]) - mean;
        vs += a * a + c * c;
      }
#pragma unroll
      for (int o = 32; o > 0; o >>= 1) vs += __shfl_xor(vs, o);
      const float rstd = 1.f / sqrtf(vs * (1.f / D_) + 1e-5f);
      unsigned* hr = (unsigned*)(ws + WS_H + (size_t)s * D_ * 2);
#pragma unroll
      for (int i = 0; i < 5; ++i) hr[lane + 64 * i] = pk_bf16((bf_lo(xv[i]) - mean) * rstd, (bf_hi(xv[i]) - mean) * rstd);
      if (lane == 0) ((float*)(ws + WS_G))[s] = 1.f / (1.f + __expf(-gs));
    }
  }
}

constexpr int L2_NG1 = 10 * 12, L2_NATT = 256;
constexpr int L2_LDS = 72 * 1024;

DI float gelu_tanh(float u) {
  const float z = 0.7978845608028654f * (u + 0.044715f * u * u * u);
  const float e = __expf(2.f * z);
  const float t = 1.f - 2.f / (e + 1.f);
  return 0.5f * u * (1.f + t);
}

DI void attn_stage(char* lds, const char* kv, int chunk, int buf, int wid, int lane) {
  const char* g = kv + (size_t)chunk * KV_CHUNK;
  char* l = lds + buf * KV_CHUNK;
#pragma unroll
  for (int r = 0; r < 3; ++r) {
    const int p = r * 8 + wid;
    if (p < 21) glds16(g + p * 1024 + lane * 16, l + p * 1024 + lane * 16);
  }
}

__global__ __launch_bounds__(512) void k_attn(const u16* __restrict__ w1, const u16* __restrict__ b1,
                                              char* __restrict__ ws) {
  extern __shared__ __attribute__((aligned(16))) char lds[];
  const int tid = threadIdx.x, lane = tid & 63, wid = tid >> 6;
  const int bid = blockIdx.x;
  if (bid < L2_NG1) {
    const int mt = bid / 12, nt = bid % 12;
    f32x16 acc[1][1] = {};
    const u16* hh = (const u16*)(ws + WS_H);
    gemm_loop<64, 128, 2, 4, false, false, 3>(acc, lds, hh + mt * 64, D_, w1 + nt * 128, F_, 32, tid);
    u16* u = (u16*)(ws + WS_U);
    const int wm = wid >> 2, wn = wid & 3, h = lane >> 5;
    const int f = nt * 128 + wn * 32 + (lane & 31);
    const float bb = bf2f(b1[f]);
#pragma unroll
    for (int r = 0; r < 16; ++r) {
      const int d = mt * 64 + wm * 32 + crow(r, h);
      u[(size_t)d * F_ + f] = f2bf(gelu_tanh(acc[0][0][r] + bb));
    }
    return;
  }
  const int s = (bid - L2_NG1) * 8 + wid;
  const int h = lane >> 5, hd = lane & 31;
  const char* kv = ws + WS_KV;
  float* red = (float*)(lds + 2 * KV_CHUNK);
  char* kconst = lds + 2 * KV_CHUNK + 64;
  attn_stage(lds, kv, 0, 0, wid, lane);
  float kmax2;
  {
    const float4 v = ((const float4*)(ws + WS_KN))[tid];
    float m = fmaxf(fmaxf(v.x, v.y), fmaxf(v.z, v.w));
#pragma unroll
    for (int o = 32; o > 0; o >>= 1) m = fmaxf(m, __shfl_xor(m, o));
    if (lane == 0) red[wid] = m;
    if (tid == 0) { ((unsigned*)kconst)[0] = 0x3f803f80u; ((unsigned*)kconst)[1] = 0x00003f80u; }
    __syncthreads();
    kmax2 = red[0];
#pragma unroll
    for (int i = 1; i < 8; ++i) kmax2 = fmaxf(kmax2, red[i]);
  }
  const u16* qr = (const u16*)(ws + WS_Q) + (size_t)s * D_ + hd * 20;
  const u32x2 qa = *(const u32x2*)(qr + 4 * h), qb = *(const u32x2*)(qr + 8 + 4 * h);
  u32x2 qc = *(const u32x2*)(qr + 16);
  float qn2 = 0.f;
  {
#pragma unroll
    for (int i = 0; i < 2; ++i) {
      qn2 += bf_lo(qa[i]) * bf_lo(qa[i]) + bf_hi(qa[i]) * bf_hi(qa[i]);
      qn2 += bf_lo(qb[i]) * bf_lo(qb[i]) + bf_hi(qb[i]) * bf_hi(qb[i]);
      if (h == 0) qn2 += bf_lo(qc[i]) * bf_lo(qc[i]) + bf_hi(qc[i]) * bf_hi(qc[i]);
    }
    qn2 += __shfl_xor(qn2, 32);
  }
  u32x4 b1u; b1u[0] = qa[0]; b1u[1] = qa[1]; b1u[2] = qb[0]; b1u[3] = qb[1];
  const bf16x8 B1 = __builtin_bit_cast(bf16x8, b1u);
  float mshift = sqrtf(qn2 * kmax2);
  const int kb_lane = h ? (int)(kconst - lds) : KV_KB_OFF + hd * 8;
  const int v_lane = KV_V_OFF + h * 336 + (hd < 20 ? hd : 20) * 16;
  f32x16 O;
  for (int pass = 0; pass < 2; ++pass) {
    if (pass == 1) {
      s16x4 B2z; { u32x2 t = qc; if (h) { t[0] = 0; t[1] = 0; } B2z = __builtin_bit_cast(s16x4, t); }
      float mx = -3.0e38f;
      attn_stage(lds, kv, 0, 0, wid, lane);
      for (int c = 0; c < 8; ++c) {
        wait_vm<0>(); raw_barrier();
        if (c + 1 < 8) attn_stage(lds, kv, c + 1, (c + 1) & 1, wid, lane);
        const char* l = lds + (c & 1) * KV_CHUNK;
        const char* lkb = h ? lds + kb_lane : l + kb_lane;
        for (int t = 0; t < 8; ++t) {
          const bf16x8 A1 = *(const bf16x8*)(l + t * 1024 + lane * 16);
          const s16x4 A2 = *(const s16x4*)(lkb + (h ? 0 : t * 256));
          f32x16 Sx = {};
          Sx = mfma32(A1, B1, Sx); Sx = mfma32k8(A2, B2z, Sx);
#pragma unroll
          for (int r = 0; r < 16; ++r) mx = fmaxf(mx, Sx[r]);
        }
      }
      mx = fmaxf(mx, __shfl_xor(mx, 32));
      mshift = mx;
      raw_barrier();
      attn_stage(lds, kv, 0, 0, wid, lane);
    }
    s16x4 B2;
    {
      const float m0 = bf2f(f2bf(mshift)), m1 = bf2f(f2bf(mshift - m0)), m2 = bf2f(f2bf(mshift - m0 - m1));
      u32x2 t = qc;
      if (h) { t[0] = pk_bf16(-m0, -m1); t[1] = pk_bf16(-m2, 0.f); }
      B2 = __builtin_bit_cast(s16x4, t);
    }
    O = f32x16{};
    for (int c = 0; c < 8; ++c) {
      wait_vm<0>(); raw_barrier();
      if (c + 1 < 8) attn_stage(lds, kv, c + 1, (c + 1) & 1, wid, lane);
      const char* l = lds + (c & 1) * KV_CHUNK;
      const char* lkb = h ? lds + kb_lane : l + kb_lane;
      const int kbs = h ? 0 : 256;
#pragma unroll
      for (int t = 0; t < 8; ++t) {
        const bf16x8 A1 = *(const bf16x8*)(l + t * 1024 + lane * 16);
        const s16x4 A2 = *(const s16x4*)(lkb + t * kbs);
        const bf16x8 V0 = *(const bf16x8*)(l + v_lane + t * 1344);
        const bf16x8 V1 = *(const bf16x8*)(l + v_lane + t * 1344 + 672);
        f32x16 Sx = {};
        Sx = mfma32(A1, B1, Sx); Sx = mfma32k8(A2, B2, Sx);
        u32x4 p0, p1;
#pragma unroll
        for (int i = 0; i < 4; ++i) {
          p0[i] = pk_bf16(__builtin_amdgcn_exp2f(Sx[2 * i]), __builtin_amdgcn_exp2f(Sx[2 * i + 1]));
          p1[i] = pk_bf16(__builtin_amdgcn_exp2f(Sx[8 + 2 * i]), __builtin_amdgcn_exp2f(Sx[8 + 2 * i + 1]));
        }
        O = mfma32(V0, __builtin_bit_cast(bf16x8, p0), O);
        O = mfma32(V1, __builtin_bit_cast(bf16x8, p1), O);
      }
    }
    const float lsum = __shfl(O[8], hd + 32);
    const bool bad = !(lsum >= 8.271806e-25f && lsum <= 3.0e38f);
    if (pass == 1) break;
    raw_barrier();
    if (tid == 0) red[8] = 0.f;
    __syncthreads();
    if (__any(bad) && lane == 0) red[8] = 1.f;
    __syncthreads();
    if (red[8] == 0.f) break;
  }
  {
    const float lsum = __shfl(O[8], hd + 32);
    const float inv = 1.f / lsum;
    u16* orow = (u16*)(ws + WS_O) + (size_t)s * D_ + hd * 20 + 4 * h;
    u32x2 a, b;
    a[0] = pk_bf16(O[0] * inv, O[1] * inv); a[1] = pk_bf16(O[2] * inv, O[3] * inv);
    b[0] = pk_bf16(O[4] * inv, O[5] * inv); b[1] = pk_bf16(O[6] * inv, O[7] * inv);
    *(u32x2*)(orow) = a;
    *(u32x2*)(orow + 8) = b;
    if (h == 0) {
      u32x2 c; c[0] = pk_bf16(O[8] * inv, O[9] * inv); c[1] = pk_bf16(O[10] * inv, O[11] * inv);
      *(u32x2*)(orow + 16) = c;
    }
  }
}

constexpr int L3_LDS = 64 * 1024;
__global__ __launch_bounds__(256) void k_final(const u16* __restrict__ wo, const u16* __restrict__ w2,
                                               const u16* __restrict__ b2, const char* __restrict__ ws,
                                               u16* __restrict__ out) {
  extern __shared__ __attribute__((aligned(16))) char lds[];
  const int tid = threadIdx.x, lane = tid & 63, wid = tid >> 6;
  const int mt = blockIdx.x / 10, nt = blockIdx.x % 10;
  f32x16 acca[1][1] = {}, accb[1][1] = {};
  const u16* o = (const u16*)(ws + WS_O);
  const u16* u = (const u16*)(ws + WS_U);
  gemm_loop<64, 64, 2, 2, false, true, 4>(accb, lds, w2 + mt * 64, S_, u + (size_t)nt * 64 * F_, F_, 24, tid);
  gemm_loop<64, 64, 2, 2, true, false, 4>(acca, lds, o + (size_t)mt * 64 * D_, D_, wo + nt * 64, D_, 10, tid);
  const float* g = (const float*)(ws + WS_G);
  const int wm = wid >> 1, wn = wid & 1, h = lane >> 5;
  const int col = nt * 64 + wn * 32 + (lane & 31);
#pragma unroll
  for (int r = 0; r < 16; ++r) {
    const int row = mt * 64 + wm * 32 + crow(r, h);
    const float gg = g[row];
    out[(size_t)row * D_ + col] = f2bf(gg * acca[0][0][r] + (1.f - gg) * (accb[0][0][r] + bf2f(b2[row])));
  }
}

extern "C" void kernel_launch(void* const* d_in, const int* in_sizes, int n_in, void* d_out, int out_size,
                              void* d_ws, size_t ws_size, hipStream_t stream) {
  const u16* x = (const u16*)d_in[0];
  const u16* wq = (const u16*)d_in[1];
  const u16* wk = (const u16*)d_in[2];
  const u16* wv = (const u16*)d_in[3];
  const u16* wo = (const u16*)d_in[4];
  const u16* w1 = (const u16*)d_in[6];
  const u16* b1 = (const u16*)d_in[7];
  const u16* w2 = (const u16*)d_in[8];
  const u16* b2 = (const u16*)d_in[9];
  const u16* gw = (const u16*)d_in[10];
  char* ws = (char*)d_ws;
  (void)hipFuncSetAttribute((const void*)k_proj, hipFuncAttributeMaxDynamicSharedMemorySize, 72 * 1024);
  (void)hipFuncSetAttribute((const void*)k_attn, hipFuncAttributeMaxDynamicSharedMemorySize, L2_LDS);
  (void)hipFuncSetAttribute((const void*)k_final, hipFuncAttributeMaxDynamicSharedMemorySize, L3_LDS);
  k_proj<<<L1_NQ + L1_NKV + L1_NLN, 256, 72 * 1024, stream>>>(x, wq, wk, wv, gw, ws);
  k_attn<<<L2_NG1 + L2_NATT, 512, L2_LDS, stream>>>(w1, b1, ws);
  k_final<<<32 * 10, 256, L3_LDS, stream>>>(wo, w2, b2, ws, (u16*)d_out);
}
```

```cpp
#include <hip/hip_runtime.h>
#include <hip/hip_bf16.h>
#include <stdint.h>

typedef unsigned short u16;
typedef __attribute__((ext_vector_type(8))) short bf16x8;
typedef __attribute__((ext_vector_type(4))) short s16x4;
typedef __attribute__((ext_vector_type(16))) float f32x16;
typedef __attribute__((ext_vector_type(4))) float f32x4;
typedef __attribute__((ext_vector_type(4))) unsigned u32x4;
typedef __attribute__((ext_vector_type(2))) unsigned u32x2;
typedef __attribute__((ext_vector_type(2))) __bf16 bf16x2_t;
#define LDS_AS __attribute__((address_space(3)))
#define DI __device__ __forceinline__
#define SB() __builtin_amdgcn_sched_barrier(0)

constexpr int S_ = 2048, D_ = 640, F_ = 1536;

constexpr size_t WS_CNT = 0;
constexpr size_t WS_Q   = 4096;
constexpr size_t WS_H   = WS_Q + (size_t)S_ * D_ * 2;
constexpr size_t WS_O   = WS_H + (size_t)S_ * D_ * 2;
constexpr size_t WS_Y   = WS_O + (size_t)S_ * D_ * 2;
constexpr size_t WS_U   = WS_Y + (size_t)S_ * D_ * 2;
constexpr size_t WS_KV  = WS_U + (size_t)D_ * F_ * 2;
constexpr int    KV_CHUNK = 23552;
constexpr int    KV_KB_OFF = 8192, KV_V_OFF = 12288;
constexpr size_t WS_KN  = WS_KV + 8 * KV_CHUNK;
constexpr size_t WS_G   = WS_KN + 256;

DI void glds16(const void* gbase, unsigned voff, const void* lds_wave_base) {
  const unsigned dst = __builtin_amdgcn_readfirstlane((unsigned)(uintptr_t)lds_wave_base);
  const unsigned lo = __builtin_amdgcn_readfirstlane((unsigned)(uintptr_t)gbase);
  const unsigned hi = __builtin_amdgcn_readfirstlane((unsigned)((uintptr_t)gbase >> 32));
  const unsigned long long gb = ((unsigned long long)hi << 32) | lo;
  unsigned keep;
  asm volatile("s_mov_b32 %0, m0\n\ts_mov_b32 m0, %3\n\ts_nop 0\n\tglobal_load_lds_dwordx4 %1, %2\n\ts_mov_b32 m0, %0"
               : "=&s"(keep) : "v"(voff), "s"(gb), "s"(dst) : "memory");
}
DI unsigned pk_bf16(float a, float b) {
  bf16x2_t p; p[0] = (__bf16)a; p[1] = (__bf16)b; return __builtin_bit_cast(unsigned, p);
}
DI float bf_lo(unsigned u) { return __uint_as_float(u << 16); }
DI float bf_hi(unsigned u) { return __uint_as_float(u & 0xffff0000u); }
DI float bf2f(u16 v) { return __uint_as_float((unsigned)v << 16); }
DI u16 f2bf(float f) { return (u16)(pk_bf16(f, 0.f) & 0xffffu); }
DI f32x16 mfma32(bf16x8 a, bf16x8 b, f32x16 c) { return __builtin_amdgcn_mfma_f32_32x32x16_bf16(a, b, c, 0, 0, 0); }
DI f32x16 mfma32k8(s16x4 a, s16x4 b, f32x16 c) { return __builtin_amdgcn_mfma_f32_32x32x8bf16_1k(a, b, c, 0, 0, 0); }
DI f32x4 mfma16(bf16x8 a, bf16x8 b, f32x4 c) { return __builtin_amdgcn_mfma_f32_16x16x32_bf16(a, b, c, 0, 0, 0); }
DI s16x4 tr_read(const char* p) { return __builtin_amdgcn_ds_read_tr16_b64_v4i16((LDS_AS s16x4*)p); }
DI bf16x8 cat8(s16x4 lo, s16x4 hi) { return __builtin_shufflevector(lo, hi, 0, 1, 2, 3, 4, 5, 6, 7); }
template <int N> DI void wait_vm() { asm volatile("s_waitcnt vmcnt(%0)" :: "i"(N) : "memory"); }
DI void raw_barrier() {
  asm volatile("s_waitcnt lgkmcnt(0)" ::: "memory");
  __builtin_amdgcn_s_barrier();
}
DI int crow(int r, int h) { return (r & 3) + 8 * (r >> 2) + 4 * h; }

template <int R, int NT> DI void stage_kc(char* lds, const u16* g, int ld, int tid) {
  constexpr int NCH = R * 8, N = (NCH + NT - 1) / NT;
#pragma unroll
  for (int i = 0; i < N; ++i) {
    const int idx = i * NT + tid;
    if ((i + 1) * NT <= NCH || (i * NT + __builtin_amdgcn_readfirstlane(tid & ~63)) < NCH) {
      const int row = idx >> 3, ch = (idx & 7) ^ ((row >> 1) & 7);
      glds16(g, (unsigned)(row * ld + ch * 8) * 2u, lds + (i * NT + __builtin_amdgcn_readfirstlane(tid & ~63)) * 16);
    }
  }
}
template <int C> DI int swz_ks(int k) { return C == 64 ? ((((k >> 1) & 1) << 2) | (((k >> 3) & 1) << 1)) : 0; }
template <int C, int NT> DI void stage_ks(char* lds, const u16* g, int ld, int tid) {
  constexpr int CPR = C / 8, NCH = 64 * CPR, N = (NCH + NT - 1) / NT;
#pragma unroll
  for (int i = 0; i < N; ++i) {
    const int idx = i * NT + tid;
    if ((i + 1) * NT <= NCH || (i * NT + __builtin_amdgcn_readfirstlane(tid & ~63)) < NCH) {
      const int k = idx / CPR, ch = (idx % CPR) ^ swz_ks<C>(k);
      glds16(g, (unsigned)(k * ld + ch * 8) * 2u, lds + (i * NT + __builtin_amdgcn_readfirstlane(tid & ~63)) * 16);
    }
  }
}
template <int NCH, int NT> constexpr int nload(int wid) { return NCH / NT + ((wid * 64 < NCH % NT) ? 1 : 0); }

DI bf16x8 frag32_ks64(const char* lds, int c0, int ks, int lane) {
  const int h = lane >> 5, blk = (lane >> 4) & 1, q = (lane & 15) >> 2, p = lane & 3;
  const int k = 16 * ks + 8 * h + q, ch = ((c0 >> 3) + 2 * blk + (p >> 1)) ^ swz_ks<64>(k);
  const char* a = lds + k * 128 + ch * 16 + 8 * (p & 1);
  return cat8(tr_read(a), tr_read(a + 4 * 128));
}
DI bf16x8 frag32_kc(const char* lds, int row0, int ks, int lane) {
  const int row = row0 + (lane & 31), ch = (2 * ks + (lane >> 5)) ^ ((row >> 1) & 7);
  return *(const bf16x8*)(lds + row * 128 + ch * 16);
}
DI bf16x8 frag16_kc(const char* lds, int row0, int ks, int lane) {
  const int row = row0 + (lane & 15), ch = (4 * ks + (lane >> 4)) ^ ((row >> 1) & 7);
  return *(const bf16x8*)(lds + row * 128 + ch * 16);
}
template <int C> DI bf16x8 frag16_ks(const char* lds, int c0, int ks, int lane) {
  const int g = lane >> 4, q = (lane & 15) >> 2, p = lane & 3;
  const int k = 32 * ks + 8 * g + q, ch = ((c0 >> 3) + (p >> 1)) ^ swz_ks<C>(k);
  const char* a = lds + k * (2 * C) + ch * 16 + 8 * (p & 1);
  return cat8(tr_read(a), tr_read(a + 4 * (2 * C)));
}

constexpr int T80_STG = 64 * 128 + 80 * 128;
template <bool AKC, bool BKC, int NT, int NBUF>
DI void gemm80(f32x4 (&acc)[5], char* lds, const u16* A, int lda, const u16* B, int ldb, int nk, int tid) {
  const int lane = tid & 63, wid = __builtin_amdgcn_readfirstlane(tid >> 6), wm = wid & 3;
  constexpr int GHI = nload<512, NT>(0) + nload<640, NT>(0), GLO = nload<512, NT>(NT / 64 - 1) + nload<640, NT>(NT / 64 - 1);
  const bool hi = (wid * 64 < 640 % NT);
  auto stage = [&](int kt, int buf) {
    char* l = lds + buf * T80_STG;
    if constexpr (AKC) stage_kc<64, NT>(l, A + kt * 64, lda, tid);
    else stage_ks<64, NT>(l, A + (size_t)kt * 64 * lda, lda, tid);
    if constexpr (BKC) stage_kc<80, NT>(l + 8192, B + kt * 64, ldb, tid);
    else stage_ks<80, NT>(l + 8192, B + (size_t)kt * 64 * ldb, ldb, tid);
  };
#pragma unroll
  for (int i = 0; i < NBUF - 1; ++i) stage(i, i);
  int buf = 0;
  for (int kt = 0; kt < nk; ++kt) {
    if (kt + NBUF - 1 <= nk) { if (hi) wait_vm<GHI * (NBUF - 2)>(); else wait_vm<GLO * (NBUF - 2)>(); }
    else wait_vm<0>();
    raw_barrier();
    if (kt + NBUF - 1 < nk) {
      int bn = buf + NBUF - 1; if (bn >= NBUF) bn -= NBUF;
      stage(kt + NBUF - 1, bn);
    }
    const char* la = lds + buf * T80_STG;
    const char* lb = la + 8192;
#pragma unroll
    for (int kk = 0; kk < (NT == 512 ? 1 : 2); ++kk) {
      const int ks = NT == 512 ? (wid >> 2) : kk;
      bf16x8 af, bfr[5];
      if constexpr (AKC) af = frag16_kc(la, wm * 16, ks, lane); else af = frag16_ks<64>(la, wm * 16, ks, lane);
#pragma unroll
      for (int j = 0; j < 5; ++j) {
        if constexpr (BKC) bfr[j] = frag16_kc(lb, j * 16, ks, lane); else bfr[j] = frag16_ks<80>(lb, j * 16, ks, lane);
      }
#pragma unroll
      for (int j = 0; j < 5; ++j) acc[j] = mfma16(af, bfr[j], acc[j]);
    }
    ++buf; if (buf == NBUF) buf = 0;
  }
  raw_barrier();
}

constexpr int L1_NQ = 256, L1_NKV = 32, L1_NLN = 64;
constexpr int L1_LDS = 4 * T80_STG;
constexpr float QSCALE = 0.22360679774997896f * 1.4426950408889634f;

__global__ __launch_bounds__(256) void k_proj(const u16* __restrict__ x, const u16* __restrict__ wq,
                                              const u16* __restrict__ wk, const u16* __restrict__ wv,
                                              const u16* __restrict__ gw, char* __restrict__ ws) {
  extern __shared__ __attribute__((aligned(16))) char lds[];
  const int tid = threadIdx.x, lane = tid & 63, wid = tid >> 6;
  const int bid = blockIdx.x;
  if (bid < L1_NQ) {
    const int mt = (bid & 7) * 4 + (bid >> 6), nt = (bid >> 3) & 7;
    f32x4 acc[5] = {};
    gemm80<true, false, 256, 4>(acc, lds, x + (size_t)mt * 64 * D_, D_, wq + nt * 80, D_, 10, tid);
    u16* t = (u16*)lds;
#pragma unroll
    for (int j = 0; j < 5; ++j)
#pragma unroll
      for (int r = 0; r < 4; ++r)
        t[(wid * 16 + (lane >> 4) * 4 + r) * 80 + j * 16 + (lane & 15)] = f2bf(acc[j][r] * QSCALE);
    __syncthreads();
    u16* q = (u16*)(ws + WS_Q) + (size_t)mt * 64 * D_ + nt * 80;
    for (int i = tid; i < 640; i += 256) {
      const int row = i / 10, ch = i % 10;
      *(u32x4*)(q + (size_t)row * D_ + ch * 8) = *(const u32x4*)(lds + i * 16);
    }
  } else if (bid < L1_NQ + L1_NKV) {
    const int b = bid - L1_NQ;
    const int mt = (b & 7) * 4 + (b >> 3);
    const u16* xa = x + (size_t)mt * 64 * D_;
    const int uw = __builtin_amdgcn_readfirstlane(wid);
    const int wm = wid >> 1, isv = wid & 1, h = lane >> 5;
    const int blk = (lane >> 4) & 1, qq = (lane & 15) >> 2, p = lane & 3;
    constexpr int KST = 14336, NBUF = 5;
    auto stage = [&](int kt, int buf) {
      char* l = lds + buf * KST;
      stage_kc<64, 256>(l, xa + kt * 64, D_, tid);
#pragma unroll
      for (int i = 0; i < 2; ++i) {
        const int pc = i * 4 + uw;
        if (pc < 6) {
          const int c = (pc % 3) * 64 + lane;
          glds16((pc < 3 ? wk : wv) + kt * 1280, (unsigned)(c < 160 ? c : 159) * 16u, l + 8192 + pc * 1024);
        }
      }
    };
    f32x16 acc = {};
#pragma unroll
    for (int i = 0; i < NBUF - 1; ++i) stage(i, i);
    int buf = 0;
    for (int kt = 0; kt < 10; ++kt) {
      if (kt + NBUF - 1 <= 10) { if (uw < 2) wait_vm<4 * (NBUF - 2)>(); else wait_vm<3 * (NBUF - 2)>(); }
      else wait_vm<0>();
      raw_barrier();
      if (kt + NBUF - 1 < 10) { int bn = buf + NBUF - 1; if (bn >= NBUF) bn -= NBUF; stage(kt + NBUF - 1, bn); }
      const char* la = lds + buf * KST;
      const char* lw = la + 8192 + isv * 3072;
#pragma unroll
      for (int ks = 0; ks < 4; ++ks) {
        const bf16x8 xf = frag32_kc(la, wm * 32, ks, lane);
        const char* a = lw + (16 * ks + 8 * h + qq) * 40 + (16 * blk + 4 * p) * 2;
        const bf16x8 wf = cat8(tr_read(a), tr_read(a + 4 * 40));
        if (isv) acc = mfma32(xf, wf, acc);
        else     acc = mfma32(wf, xf, acc);
      }
      ++buf; if (buf == NBUF) buf = 0;
    }
    const int tile = mt * 2 + wm;
    char* img = ws + WS_KV + (size_t)(tile >> 3) * KV_CHUNK;
    const int tl = tile & 7;
    float* red = (float*)lds;
    __syncthreads();
    if (!isv) {
      u32x4 a; a[0] = pk_bf16(acc[0], acc[1]); a[1] = pk_bf16(acc[2], acc[3]);
      a[2] = pk_bf16(acc[4], acc[5]); a[3] = pk_bf16(acc[6], acc[7]);
      *(u32x4*)(img + tl * 1024 + lane * 16) = a;
      float n2 = 0.f;
#pragma unroll
      for (int i = 0; i < 4; ++i) { float lo = bf_lo(a[i]), hi = bf_hi(a[i]); n2 += lo * lo + hi * hi; }
      u32x2 bb;
      if (h == 0) {
        bb[0] = pk_bf16(acc[8], acc[9]); bb[1] = pk_bf16(acc[10], acc[11]);
#pragma unroll
        for (int i = 0; i < 2; ++i) { float lo = bf_lo(bb[i]), hi = bf_hi(bb[i]); n2 += lo * lo + hi * hi; }
      } else { bb[0] = 0x3f803f80u; bb[1] = 0x00003f80u; }
      *(u32x2*)(img + KV_KB_OFF + tl * 512 + lane * 8) = bb;
      n2 += __shfl_xor(n2, 32);
#pragma unroll
      for (int o = 16; o > 0; o >>= 1) n2 = fmaxf(n2, __shfl_xor(n2, o));
      if (lane == 0) red[wm] = n2;
    } else {
      const int d = lane & 31;
      if (d <= 20) {
#pragma unroll
        for (int s = 0; s < 2; ++s) {
          u32x4 a;
#pragma unroll
          for (int i = 0; i < 4; ++i) a[i] = d < 20 ? pk_bf16(acc[8 * s + 2 * i], acc[8 * s + 2 * i + 1]) : 0x3f803f80u;
          *(u32x4*)(img + KV_V_OFF + tl * 1344 + (s * 2 + h) * 336 + d * 16) = a;
        }
      }
    }
    __syncthreads();
    if (tid == 0) ((float*)(ws + WS_KN))[mt] = fmaxf(red[0], red[1]);
    if (b == 0 && tid < 16) ((unsigned*)(ws + WS_CNT))[tid * 32] = 0u;
  } else {
    const int b = bid - L1_NQ - L1_NKV;
    unsigned gwv[5];
#pragma unroll
    for (int i = 0; i < 5; ++i) gwv[i] = ((const unsigned*)gw)[lane + 64 * i];
#pragma unroll 2
    for (int t = 0; t < 8; ++t) {
      const int s = (b & 7) * 256 + (b >> 3) * 32 + wid * 8 + t;
      const unsigned* xr = (const unsigned*)(x + (size_t)s * D_);
      unsigned xv[5];
#pragma unroll
      for (int i = 0; i < 5; ++i) xv[i] = xr[lane + 64 * i];
      float sum = 0.f, gs = 0.f;
#pragma unroll
      for (int i = 0; i < 5; ++i) {
        sum += bf_lo(xv[i]) + bf_hi(xv[i]);
        gs += bf_lo(xv[i]) * bf_lo(gwv[i]) + bf_hi(xv[i]) * bf_hi(gwv[i]);
      }
#pragma unroll
      for (int o = 32; o > 0; o >>= 1) { sum += __shfl_xor(sum, o); gs += __shfl_xor(gs, o); }
      const float mean = sum * (1.f / D_);
      float vs = 0.f;
#pragma unroll
      for (int i = 0; i < 5; ++i) {
        const float a = bf_lo(xv[i]) - mean, c = bf_hi(xv[i]) - mean;
        vs += a * a + c * c;
      }
#pragma unroll
      for (int o = 32; o > 0; o >>= 1) vs += __shfl_xor(vs, o);
      const float rstd = 1.f / sqrtf(vs * (1.f / D_) + 1e-5f);
      unsigned* hr = (unsigned*)(ws + WS_H + (size_t)s * D_ * 2);
#pragma unroll
      for (int i = 0; i < 5; ++i) hr[lane + 64 * i] = pk_bf16((bf_lo(xv[i]) - mean) * rstd, (bf_hi(xv[i]) - mean) * rstd);
      if (lane == 0) ((float*)(ws + WS_G))[s] = 1.f / (1.f + __expf(-gs));
    }
  }
}

constexpr int L2_LDS = 4 * T80_STG;

DI float gelu_tanh(float u) {
  const float z = 0.7978845608028654f * (u + 0.044715f * u * u * u);
  const float e = __expf(2.f * z);
  const float t = 1.f - 2.f / (e + 1.f);
  return 0.5f * u * (1.f + t);
}

DI void attn_stage(char* lds, const char* kv, int chunk, int buf, int wid, int lane) {
  const char* g = kv + (size_t)chunk * KV_CHUNK;
  char* l = lds + buf * KV_CHUNK;
#pragma unroll
  for (int r = 0; r < 3; ++r) {
    const int p = r * 8 + wid;
    if (p < 23) glds16(g + p * 1024, (unsigned)lane * 16u, l + p * 1024);
  }
}

typedef __attribute__((address_space(1))) unsigned gu32;

__global__ __launch_bounds__(512, 4) void k_main(const u16* __restrict__ w1, const u16* __restrict__ b1,
                                                 const u16* __restrict__ w2, const u16* __restrict__ b2,
                                                 char* __restrict__ ws) {
  extern __shared__ __attribute__((aligned(16))) char lds[];
  const int tid = threadIdx.x, lane = tid & 63, wid = __builtin_amdgcn_readfirstlane(tid >> 6);
  const int bid = blockIdx.x;
  if (bid < 256) {
    const int xcd = bid & 7, i = bid >> 3;
    gu32* cnt = (gu32*)(ws + WS_CNT);
    if (i < 30) {
      const int mt = i / 3, nt = xcd * 3 + i % 3;
      const u16* A = (const u16*)(ws + WS_H) + mt * 64;
      const u16* B = w1 + nt * 64;
      const int wm = (wid >> 1) & 1, wn = wid & 1, kg = wid >> 2;
      f32x16 acc = {};
      constexpr int NBUF = 4, NK = 32;
      auto stage = [&](int kt, int buf) {
        char* l = lds + buf * 16384;
        stage_ks<64, 512>(l, A + (size_t)kt * 64 * D_, D_, tid);
        stage_ks<64, 512>(l + 8192, B + (size_t)kt * 64 * F_, F_, tid);
      };
#pragma unroll
      for (int j = 0; j < NBUF - 1; ++j) stage(j, j);
      for (int kt = 0; kt < NK; ++kt) {
        if (kt + NBUF - 1 <= NK) wait_vm<2 * (NBUF - 2)>(); else wait_vm<0>();
        raw_barrier();
        if (kt + NBUF - 1 < NK) stage(kt + NBUF - 1, (kt + NBUF - 1) & 3);
        const char* la = lds + (kt & 3) * 16384;
        const bf16x8 a0 = frag32_ks64(la, wm * 32, 2 * kg, lane), a1 = frag32_ks64(la, wm * 32, 2 * kg + 1, lane);
        const bf16x8 b0 = frag32_ks64(la + 8192, wn * 32, 2 * kg, lane), bb1 = frag32_ks64(la + 8192, wn * 32, 2 * kg + 1, lane);
        acc = mfma32(a0, b0, acc);
        acc = mfma32(a1, bb1, acc);
      }
      raw_barrier();
      float* rb = (float*)lds;
      if (kg == 1) {
#pragma unroll
        for (int r = 0; r < 16; ++r) rb[((wid & 3) * 16 + r) * 64 + lane] = acc[r];
      }
      __syncthreads();
      u16* t = (u16*)(lds + 16384);
      if (kg == 0) {
        const int h = lane >> 5, fl = wn * 32 + (lane & 31);
        const float bb = bf2f(b1[nt * 64 + fl]);
#pragma unroll
        for (int r = 0; r < 16; ++r)
          t[(wm * 32 + crow(r, h)) * 64 + fl] = f2bf(gelu_tanh(acc[r] + rb[((wid & 3) * 16 + r) * 64 + lane] + bb));
      }
      __syncthreads();
      {
        const int row = tid >> 3, ch = tid & 7;
        const u32x4 v = *(const u32x4*)(lds + 16384 + tid * 16);
        u16* dst = (u16*)(ws + WS_U) + (size_t)(mt * 64 + row) * F_ + nt * 64 + ch * 8;
        asm volatile("global_store_dwordx4 %0, %1, off sc1\n\ts_nop 1" :: "v"(dst), "v"(v) : "memory");
      }
      wait_vm<0>();
      __syncthreads();
      if (tid == 0) __hip_atomic_fetch_add(cnt + mt * 32, 1u, __ATOMIC_RELAXED, __HIP_MEMORY_SCOPE_AGENT);
    }
    if (wid == 0) {
      for (int spin = 0; spin < (1 << 22); ++spin) {
        const unsigned c = __hip_atomic_load(cnt + (lane < 10 ? lane : 0) * 32, __ATOMIC_RELAXED, __HIP_MEMORY_SCOPE_AGENT);
        if (__all(c >= 24u)) break;
        __builtin_amdgcn_s_sleep(2);
      }
      __builtin_amdgcn_fence(__ATOMIC_ACQUIRE, "agent");
      wait_vm<0>();
    }
    __syncthreads();
    {
      const int mt = xcd * 4 + (i >> 3), nt = i & 7;
      f32x4 acc[5] = {};
      gemm80<false, true, 512, 4>(acc, lds, w2 + mt * 64, S_, (const u16*)(ws + WS_U) + (size_t)nt * 80 * F_, F_, 24, tid);
      float* rb = (float*)lds;
      if (wid >= 4) {
#pragma unroll
        for (int j = 0; j < 5; ++j)
#pragma unroll
          for (int r = 0; r < 4; ++r) rb[((wid & 3) * 20 + j * 4 + r) * 64 + lane] = acc[j][r];
      }
      __syncthreads();
      u16* t = (u16*)(lds + 20480);
      if (wid < 4) {
#pragma unroll
        for (int r = 0; r < 4; ++r) {
          const int row = wid * 16 + (lane >> 4) * 4 + r;
          const float bb = bf2f(b2[mt * 64 + row]);
#pragma unroll
          for (int j = 0; j < 5; ++j)
            t[row * 80 + j * 16 + (lane & 15)] = f2bf(acc[j][r] + rb[(wid * 20 + j * 4 + r) * 64 + lane] + bb);
        }
      }
      __syncthreads();
      u16* y = (u16*)(ws + WS_Y) + (size_t)mt * 64 * D_ + nt * 80;
      for (int c = tid; c < 640; c += 512) {
        const int row = c / 10, ch = c % 10;
        *(u32x4*)(y + (size_t)row * D_ + ch * 8) = *(const u32x4*)(lds + 20480 + c * 16);
      }
    }
    return;
  }
  const int ab = bid - 256;
  const int s = ((ab & 7) * 32 + (ab >> 3)) * 8 + wid;
  const int h = lane >> 5, hd = lane & 31;
  const char* kv = ws + WS_KV;
  float* red = (float*)(lds + 3 * KV_CHUNK);
  const u16* qr = (const u16*)(ws + WS_Q) + (size_t)s * D_ + hd * 20;
  const u32x2 qa = *(const u32x2*)(qr + 4 * h), qb = *(const u32x2*)(qr + 8 + 4 * h);
  const u32x2 qc = *(const u32x2*)(qr + 16);
  float kmax2 = ((const float*)(ws + WS_KN))[hd];
  attn_stage(lds, kv, 0, 0, wid, lane);
  attn_stage(lds, kv, 1, 1, wid, lane);
#pragma unroll
  for (int o = 16; o > 0; o >>= 1) kmax2 = fmaxf(kmax2, __shfl_xor(kmax2, o));
  float qn2 = 0.f;
#pragma unroll
  for (int i = 0; i < 2; ++i) {
    qn2 += bf_lo(qa[i]) * bf_lo(qa[i]) + bf_hi(qa[i]) * bf_hi(qa[i]);
    qn2 += bf_lo(qb[i]) * bf_lo(qb[i]) + bf_hi(qb[i]) * bf_hi(qb[i]);
    if (h == 0) qn2 += bf_lo(qc[i]) * bf_lo(qc[i]) + bf_hi(qc[i]) * bf_hi(qc[i]);
  }
  qn2 += __shfl_xor(qn2, 32);
  u32x4 b1u; b1u[0] = qa[0]; b1u[1] = qa[1]; b1u[2] = qb[0]; b1u[3] = qb[1];
  const bf16x8 B1 = __builtin_bit_cast(bf16x8, b1u);
  float mshift = sqrtf(qn2 * kmax2);
  const int ka_lane = lane * 16, kb_lane = KV_KB_OFF + lane * 8;
  const int v_lane = KV_V_OFF + h * 336 + (hd < 20 ? hd : 20) * 16;
  f32x16 O;
  for (int pass = 0; pass < 2; ++pass) {
    if (pass == 1) {
      s16x4 B2z; { u32x2 t = qc; if (h) { t[0] = 0; t[1] = 0; } B2z = __builtin_bit_cast(s16x4, t); }
      float mx = -3.0e38f;
      for (int c = 0; c < 8; ++c) {
        attn_stage(lds, kv, c, 0, wid, lane);
        wait_vm<0>(); raw_barrier();
        for (int t = 0; t < 8; ++t) {
          const bf16x8 A1 = *(const bf16x8*)(lds + t * 1024 + ka_lane);
          const s16x4 A2 = *(const s16x4*)(lds + t * 512 + kb_lane);
          f32x16 Sx = {};
          Sx = mfma32(A1, B1, Sx); Sx = mfma32k8(A2, B2z, Sx);
#pragma unroll
          for (int r = 0; r < 16; ++r) mx = fmaxf(mx, Sx[r]);
        }
        raw_barrier();
      }
      mx = fmaxf(mx, __shfl_xor(mx, 32));
      mshift = mx;
      attn_stage(lds, kv, 0, 0, wid, lane);
      attn_stage(lds, kv, 1, 1, wid, lane);
    }
    s16x4 B2;
    {
      const float m0 = bf2f(f2bf(mshift)), m1 = bf2f(f2bf(mshift - m0)), m2 = bf2f(f2bf(mshift - m0 - m1));
      u32x2 t = qc;
      if (h) { t[0] = pk_bf16(-m0, -m1); t[1] = pk_bf16(-m2, 0.f); }
      B2 = __builtin_bit_cast(s16x4, t);
    }
    O = f32x16{};
    f32x16 Sa, Sb;
    bf16x8 KA1; s16x4 KA2;
    auto tile = [&](const f32x16& Sc, f32x16& Sn, const char* vp, const char* k2a, const char* k2b) {
      const bf16x8 V0 = *(const bf16x8*)(vp), V1 = *(const bf16x8*)(vp + 672);
      u32x4 p0, p1;
      p0[0] = pk_bf16(__builtin_amdgcn_exp2f(Sc[0]), __builtin_amdgcn_exp2f(Sc[1]));
      p0[1] = pk_bf16(__builtin_amdgcn_exp2f(Sc[2]), __builtin_amdgcn_exp2f(Sc[3]));
      SB();
      Sn = mfma32(KA1, B1, f32x16{});
      SB();
      p0[2] = pk_bf16(__builtin_amdgcn_exp2f(Sc[4]), __builtin_amdgcn_exp2f(Sc[5]));
      p0[3] = pk_bf16(__builtin_amdgcn_exp2f(Sc[6]), __builtin_amdgcn_exp2f(Sc[7]));
      SB();
      Sn = mfma32k8(KA2, B2, Sn);
      KA1 = *(const bf16x8*)(k2a); KA2 = *(const s16x4*)(k2b);
      SB();
      p1[0] = pk_bf16(__builtin_amdgcn_exp2f(Sc[8]), __builtin_amdgcn_exp2f(Sc[9]));
      p1[1] = pk_bf16(__builtin_amdgcn_exp2f(Sc[10]), __builtin_amdgcn_exp2f(Sc[11]));
      SB();
      O = mfma32(V0, __builtin_bit_cast(bf16x8, p0), O);
      SB();
      p1[2] = pk_bf16(__builtin_amdgcn_exp2f(Sc[12]), __builtin_amdgcn_exp2f(Sc[13]));
      p1[3] = pk_bf16(__builtin_amdgcn_exp2f(Sc[14]), __builtin_amdgcn_exp2f(Sc[15]));
      SB();
      O = mfma32(V1, __builtin_bit_cast(bf16x8, p1), O);
      SB();
    };
    int bc = 0;
    for (int c = 0; c < 8; ++c) {
      wait_vm<0>(); raw_barrier();
      int bn = bc + 1; if (bn == 3) bn = 0;
      int b2n = bn + 1; if (b2n == 3) b2n = 0;
      if (c + 2 < 8) attn_stage(lds, kv, c + 2, b2n, wid, lane);
      const char* l = lds + bc * KV_CHUNK;
      const char* ln = lds + bn * KV_CHUNK;
      if (c == 0) {
        const bf16x8 A1 = *(const bf16x8*)(l + ka_lane);
        const s16x4 A2 = *(const s16x4*)(l + kb_lane);
        KA1 = *(const bf16x8*)(l + 1024 + ka_lane); KA2 = *(const s16x4*)(l + 512 + kb_lane);
        Sa = mfma32(A1, B1, f32x16{}); Sa = mfma32k8(A2, B2, Sa);
      }
#pragma unroll
      for (int t = 0; t < 8; t += 2) {
        const char* ka = t + 2 < 8 ? l + (t + 2) * 1024 : ln + (t - 6) * 1024;
        const char* kb = t + 2 < 8 ? l + (t + 2) * 512 : ln + (t - 6) * 512;
        tile(Sa, Sb, l + v_lane + t * 1344, ka + ka_lane, kb + kb_lane);
        tile(Sb, Sa, l + v_lane + (t + 1) * 1344, ka + 1024 + ka_lane, kb + 512 + kb_lane);
      }
      bc = bn;
    }
    const float lsum = __shfl(O[8], hd + 32);
    const bool bad = !(lsum >= 8.271806e-25f && lsum <= 3.0e38f);
    if (pass == 1) break;
    raw_barrier();
    if (tid == 0) red[0] = 0.f;
    __syncthreads();
    if (__any(bad) && lane == 0) red[0] = 1.f;
    __syncthreads();
    if (red[0] == 0.f) break;
    __syncthreads();
  }
  {
    const float lsum = __shfl(O[8], hd + 32);
    const float inv = 1.f / lsum;
    u16* orow = (u16*)(ws + WS_O) + (size_t)s * D_ + hd * 20 + 4 * h;
    u32x2 a, b;
    a[0] = pk_bf16(O[0] * inv, O[1] * inv); a[1] = pk_bf16(O[2] * inv, O[3] * inv);
    b[0] = pk_bf16(O[4] * inv, O[5] * inv); b[1] = pk_bf16(O[6] * inv, O[7] * inv);
    *(u32x2*)(orow) = a;
    *(u32x2*)(orow + 8) = b;
    if (h == 0) {
      u32x2 c; c[0] = pk_bf16(O[8] * inv, O[9] * inv); c[1] = pk_bf16(O[10] * inv, O[11] * inv);
      *(u32x2*)(orow + 16) = c;
    }
  }
}

constexpr int L3_LDS = 4 * T80_STG;
__global__ __launch_bounds__(256) void k_final(const u16* __restrict__ wo, const char* __restrict__ ws,
                                               u16* __restrict__ out) {
  extern __shared__ __attribute__((aligned(16))) char lds[];
  const int tid = threadIdx.x, lane = tid & 63, wid = tid >> 6;
  const int bid = blockIdx.x;
  const int mt = (bid & 7) * 4 + (bid >> 6), nt = (bid >> 3) & 7;
  f32x4 acc[5] = {};
  gemm80<true, false, 256, 4>(acc, lds, (const u16*)(ws + WS_O) + (size_t)mt * 64 * D_, D_, wo + nt * 80, D_, 10, tid);
  float* t = (float*)lds;
#pragma unroll
  for (int j = 0; j < 5; ++j)
#pragma unroll
    for (int r = 0; r < 4; ++r) t[(wid * 16 + (lane >> 4) * 4 + r) * 84 + j * 16 + (lane & 15)] = acc[j][r];
  __syncthreads();
  const float* g = (const float*)(ws + WS_G) + mt * 64;
  const u16* y = (const u16*)(ws + WS_Y) + (size_t)mt * 64 * D_ + nt * 80;
  u16* o = out + (size_t)mt * 64 * D_ + nt * 80;
  for (int i = tid; i < 640; i += 256) {
    const int row = i / 10, ch = i % 10;
    const u32x4 yv = *(const u32x4*)(y + (size_t)row * D_ + ch * 8);
    const float gg = g[row];
    const f32x4 a0 = *(const f32x4*)(t + row * 84 + ch * 8), a1 = *(const f32x4*)(t + row * 84 + ch * 8 + 4);
    u32x4 r;
    r[0] = pk_bf16(gg * a0[0] + (1.f - gg) * bf_lo(yv[0]), gg * a0[1] + (1.f - gg) * bf_hi(yv[0]));
    r[1] = pk_bf16(gg * a0[2] + (1.f - gg) * bf_lo(yv[1]), gg * a0[3] + (1.f - gg) * bf_hi(yv[1]));
    r[2] = pk_bf16(gg * a1[0] + (1.f - gg) * bf_lo(yv[2]), gg * a1[1] + (1.f - gg) * bf_hi(yv[2]));
    r[3] = pk_bf16(gg * a1[2] + (1.f - gg) * bf_lo(yv[3]), gg * a1[3] + (1.f - gg) * bf_hi(yv[3]));
    *(u32x4*)(o + (size_t)row * D_ + ch * 8) = r;
  }
}

extern "C" void kernel_launch(void* const* d_in, const int* in_sizes, int n_in, void* d_out, int out_size,
                              void* d_ws, size_t ws_size, hipStream_t stream) {
  const u16* x = (const u16*)d_in[0];
  const u16* wq = (const u16*)d_in[1];
  const u16* wk = (const u16*)d_in[2];
  const u16* wv = (const u16*)d_in[3];
  const u16* wo = (const u16*)d_in[4];
  const u16* w1 = (const u16*)d_in[6];
  const u16* b1 = (const u16*)d_in[7];
  const u16* w2 = (const u16*)d_in[8];
  const u16* b2 = (const u16*)d_in[9];
  const u16* gw = (const u16*)d_in[10];
  char* ws = (char*)d_ws;
  (void)hipFuncSetAttribute((const void*)k_proj, hipFuncAttributeMaxDynamicSharedMemorySize, L1_LDS);
  (void)hipFuncSetAttribute((const void*)k_main, hipFuncAttributeMaxDynamicSharedMemorySize, L2_LDS);
  (void)hipFuncSetAttribute((const void*)k_final, hipFuncAttributeMaxDynamicSharedMemorySize, L3_LDS);
  k_proj<<<L1_NQ + L1_NKV + L1_NLN, 256, L1_LDS, stream>>>(x, wq, wk, wv, gw, ws);
  k_main<<<512, 512, L2_LDS, stream>>>(w1, b1, w2, b2, ws);
  k_final<<<256, 256, L3_LDS, stream>>>(wo, ws, (u16*)d_out);
}
```

```cpp
#include <hip/hip_runtime.h>
#include <hip/hip_bf16.h>
#include <stdint.h>

typedef unsigned short u16;
typedef __attribute__((ext_vector_type(8))) short bf16x8;
typedef __attribute__((ext_vector_type(4))) short s16x4;
typedef __attribute__((ext_vector_type(16))) float f32x16;
typedef __attribute__((ext_vector_type(4))) float f32x4;
typedef __attribute__((ext_vector_type(4))) unsigned u32x4;
typedef __attribute__((ext_vector_type(2))) unsigned u32x2;
typedef __attribute__((ext_vector_type(2))) __bf16 bf16x2_t;
#define LDS_AS __attribute__((address_space(3)))
#define DI __device__ __forceinline__
#define SB() __builtin_amdgcn_sched_barrier(0)

constexpr int S_ = 2048, D_ = 640, F_ = 1536;
#define STAMP(ws, kid, slot) do {} while (0)

constexpr size_t WS_CNT = 0;
constexpr size_t WS_Q   = 4096;
constexpr size_t WS_H   = WS_Q + (size_t)S_ * D_ * 2;
constexpr size_t WS_O   = WS_H + (size_t)S_ * D_ * 2;
constexpr size_t WS_Y   = WS_O + (size_t)S_ * D_ * 2;
constexpr size_t WS_U   = WS_Y + (size_t)S_ * D_ * 2;
constexpr size_t WS_KV  = WS_U + (size_t)D_ * F_ * 2;
constexpr int    KV_CHUNK = 23552;
constexpr int    KV_KB_OFF = 8192, KV_V_OFF = 12288;
constexpr size_t WS_KN  = WS_KV + 8 * KV_CHUNK;
constexpr size_t WS_G   = WS_KN + 256;

template <bool SC1 = false>
DI void glds16(const void* gbase, unsigned voff, const void* lds_wave_base) {
  const unsigned dst = __builtin_amdgcn_readfirstlane((unsigned)(uintptr_t)lds_wave_base);
  const unsigned lo = __builtin_amdgcn_readfirstlane((unsigned)(uintptr_t)gbase);
  const unsigned hi = __builtin_amdgcn_readfirstlane((unsigned)((uintptr_t)gbase >> 32));
  const unsigned long long gb = ((unsigned long long)hi << 32) | lo;
  unsigned keep;
  if constexpr (SC1)
    asm volatile("s_mov_b32 %0, m0\n\ts_mov_b32 m0, %3\n\ts_nop 0\n\tglobal_load_lds_dwordx4 %1, %2 sc1\n\ts_mov_b32 m0, %0"
                 : "=&s"(keep) : "v"(voff), "s"(gb), "s"(dst) : "memory");
  else
    asm volatile("s_mov_b32 %0, m0\n\ts_mov_b32 m0, %3\n\ts_nop 0\n\tglobal_load_lds_dwordx4 %1, %2\n\ts_mov_b32 m0, %0"
                 : "=&s"(keep) : "v"(voff), "s"(gb), "s"(dst) : "memory");
}
DI unsigned pk_bf16(float a, float b) {
  bf16x2_t p; p[0] = (__bf16)a; p[1] = (__bf16)b; return __builtin_bit_cast(unsigned, p);
}
DI float bf_lo(unsigned u) { return __uint_as_float(u << 16); }
DI float bf_hi(unsigned u) { return __uint_as_float(u & 0xffff0000u); }
DI float bf2f(u16 v) { return __uint_as_float((unsigned)v << 16); }
DI u16 f2bf(float f) { return (u16)(pk_bf16(f, 0.f) & 0xffffu); }
DI f32x16 mfma32(bf16x8 a, bf16x8 b, f32x16 c) { return __builtin_amdgcn_mfma_f32_32x32x16_bf16(a, b, c, 0, 0, 0); }
DI f32x16 mfma32k8(s16x4 a, s16x4 b, f32x16 c) { return __builtin_amdgcn_mfma_f32_32x32x8bf16_1k(a, b, c, 0, 0, 0); }
DI f32x4 mfma16(bf16x8 a, bf16x8 b, f32x4 c) { return __builtin_amdgcn_mfma_f32_16x16x32_bf16(a, b, c, 0, 0, 0); }
DI s16x4 tr_read(const char* p) { return __builtin_amdgcn_ds_read_tr16_b64_v4i16((LDS_AS s16x4*)p); }
DI bf16x8 cat8(s16x4 lo, s16x4 hi) { return __builtin_shufflevector(lo, hi, 0, 1, 2, 3, 4, 5, 6, 7); }
template <int N> DI void wait_vm() { asm volatile("s_waitcnt vmcnt(%0)" :: "i"(N) : "memory"); }
DI void raw_barrier() {
  asm volatile("s_waitcnt lgkmcnt(0)" ::: "memory");
  __builtin_amdgcn_s_barrier();
  asm volatile("" ::: "memory");
}
DI int crow(int r, int h) { return (r & 3) + 8 * (r >> 2) + 4 * h; }

template <int R, int NT, bool SC1 = false> DI void stage_kc(char* lds, const u16* g, int ld, int tid) {
  constexpr int NCH = R * 8, N = (NCH + NT - 1) / NT;
#pragma unroll
  for (int i = 0; i < N; ++i) {
    const int idx = i * NT + tid;
    if ((i + 1) * NT <= NCH || (i * NT + __builtin_amdgcn_readfirstlane(tid & ~63)) < NCH) {
      const int row = idx >> 3, ch = (idx & 7) ^ ((row >> 1) & 7);
      glds16<SC1>(g, (unsigned)(row * ld + ch * 8) * 2u, lds + (i * NT + __builtin_amdgcn_readfirstlane(tid & ~63)) * 16);
    }
  }
}
template <int C> DI int swz_ks(int k) { return C == 64 ? ((((k >> 1) & 1) << 2) | (((k >> 3) & 1) << 1)) : 0; }
template <int C, int NT> DI void stage_ks(char* lds, const u16* g, int ld, int tid) {
  constexpr int CPR = C / 8, NCH = 64 * CPR, N = (NCH + NT - 1) / NT;
#pragma unroll
  for (int i = 0; i < N; ++i) {
    const int idx = i * NT + tid;
    if ((i + 1) * NT <= NCH || (i * NT + __builtin_amdgcn_readfirstlane(tid & ~63)) < NCH) {
      const int k = idx / CPR, ch = (idx % CPR) ^ swz_ks<C>(k);
      glds16(g, (unsigned)(k * ld + ch * 8) * 2u, lds + (i * NT + __builtin_amdgcn_readfirstlane(tid & ~63)) * 16);
    }
  }
}
template <int KIND> DI unsigned piece_voff(int p, int lane, int ld) {
  const int idx = p * 64 + lane;
  if constexpr (KIND == 0) { const int row = idx >> 3, ch = (idx & 7) ^ ((row >> 1) & 7); return (unsigned)(row * ld + ch * 8) * 2u; }
  else if constexpr (KIND == 1) { const int k = idx >> 3, ch = (idx & 7) ^ swz_ks<64>(k); return (unsigned)(k * ld + ch * 8) * 2u; }
  else { const int k = idx / 10, ch = idx % 10; return (unsigned)(k * ld + ch * 8) * 2u; }
}
template <int AK, int BKD, int NPA, int NPB, bool BSC1>
struct Stager {
  static constexpr int NP = NPA + NPB, BASE = NP / 4, EXTRA = NP % 4, NJ = BASE + (EXTRA ? 1 : 0);
  unsigned voff[NJ]; int p0, n; const u16* base[NJ]; int step[NJ];
  DI void init(const u16* A, int lda, const u16* B, int ldb, int w, int lane) {
    n = BASE + (w < EXTRA ? 1 : 0); p0 = w * BASE + (w < EXTRA ? w : EXTRA);
#pragma unroll
    for (int j = 0; j < NJ; ++j) {
      const int pj = p0 + j;
      const bool isa = pj < NPA;
      voff[j] = isa ? piece_voff<AK>(pj, lane, lda) : piece_voff<BKD>(pj - NPA, lane, ldb);
      base[j] = isa ? A : B;
      step[j] = isa ? (AK == 0 ? 64 : 64 * lda) : (BKD == 0 ? 64 : 64 * ldb);
    }
  }
  DI void stage(char* l, int kt) const {
#pragma unroll
    for (int j = 0; j < NJ; ++j)
      if (j < BASE || j < n) glds16<BSC1>(base[j] + (size_t)kt * step[j], voff[j], l + (p0 + j) * 1024);
  }
  template <int M> DI void wait() const {
    if (n == BASE) wait_vm<BASE * M>(); else wait_vm<(BASE + 1) * M>();
  }
  template <int NBUF, int STG> DI void run(char* lds, int nk) const {
#pragma unroll
    for (int i = 0; i < NBUF - 1; ++i) stage(lds + i * STG, i);
    int bn = NBUF - 1;
    for (int kt = 0; kt < nk; ++kt) {
      if (kt + NBUF - 1 <= nk) wait<NBUF - 2>(); else wait_vm<0>();
      __builtin_amdgcn_s_barrier();
      if (kt + NBUF - 1 < nk) stage(lds + bn * STG, kt + NBUF - 1);
      ++bn; if (bn == NBUF) bn = 0;
    }
  }
};

DI bf16x8 frag32_ks64(const char* lds, int c0, int ks, int lane) {
  const int h = lane >> 5, blk = (lane >> 4) & 1, q = (lane & 15) >> 2, p = lane & 3;
  const int k = 16 * ks + 8 * h + q, ch = ((c0 >> 3) + 2 * blk + (p >> 1)) ^ swz_ks<64>(k);
  const char* a = lds + k * 128 + ch * 16 + 8 * (p & 1);
  return cat8(tr_read(a), tr_read(a + 4 * 128));
}
DI bf16x8 frag32_kc(const char* lds, int row0, int ks, int lane) {
  const int row = row0 + (lane & 31), ch = (2 * ks + (lane >> 5)) ^ ((row >> 1) & 7);
  return *(const bf16x8*)(lds + row * 128 + ch * 16);
}
DI bf16x8 frag16_kc(const char* lds, int row0, int ks, int lane) {
  const int row = row0 + (lane & 15), ch = (4 * ks + (lane >> 4)) ^ ((row >> 1) & 7);
  return *(const bf16x8*)(lds + row * 128 + ch * 16);
}
template <int C> DI bf16x8 frag16_ks(const char* lds, int c0, int ks, int lane) {
  const int g = lane >> 4, q = (lane & 15) >> 2, p = lane & 3;
  const int k = 32 * ks + 8 * g + q, ch = ((c0 >> 3) + (p >> 1)) ^ swz_ks<C>(k);
  const char* a = lds + k * (2 * C) + ch * 16 + 8 * (p & 1);
  return cat8(tr_read(a), tr_read(a + 4 * (2 * C)));
}

constexpr int T80_STG = 64 * 128 + 80 * 128;
template <bool AKC, bool BKC, int NBUF, bool BSC1 = false>
DI void gemm80(f32x4 (&acc)[5], char* lds, const u16* A, int lda, const u16* B, int ldb, int nk, int tid) {
  const int lane = tid & 63, wid = __builtin_amdgcn_readfirstlane(tid >> 6);
  if (wid >= 4) {
    Stager<AKC ? 0 : 1, BKC ? 0 : 2, 8, 10, BSC1> st;
    st.init(A, lda, B, ldb, wid - 4, lane);
    st.template run<NBUF, T80_STG>(lds, nk);
  } else {
    int buf = 0;
    for (int kt = 0; kt < nk; ++kt) {
      raw_barrier();
      bf16x8 af[2], bfr[2][5];
      const char* la = lds + buf * T80_STG;
      const char* lb = la + 8192;
#pragma unroll
      for (int ks = 0; ks < 2; ++ks) {
        if constexpr (AKC) af[ks] = frag16_kc(la, wid * 16, ks, lane); else af[ks] = frag16_ks<64>(la, wid * 16, ks, lane);
#pragma unroll
        for (int j = 0; j < 5; ++j) {
          if constexpr (BKC) bfr[ks][j] = frag16_kc(lb, j * 16, ks, lane); else bfr[ks][j] = frag16_ks<80>(lb, j * 16, ks, lane);
        }
      }
#pragma unroll
      for (int ks = 0; ks < 2; ++ks)
#pragma unroll
        for (int j = 0; j < 5; ++j) acc[j] = mfma16(af[ks], bfr[ks][j], acc[j]);
      ++buf; if (buf == NBUF) buf = 0;
    }
  }
  raw_barrier();
}

constexpr int L1_NQ = 256, L1_NKV = 32, L1_NLN = 64, L1_NPF = 128;
constexpr int L1_LDS = 4 * T80_STG;
constexpr float QSCALE = 0.22360679774997896f * 1.4426950408889634f;

__global__ __launch_bounds__(512) void k_proj(const u16* __restrict__ x, const u16* __restrict__ wq,
                                              const u16* __restrict__ wk, const u16* __restrict__ wv,
                                              const u16* __restrict__ gw, const u16* __restrict__ w1,
                                              const u16* __restrict__ w2, const u16* __restrict__ wo,
                                              char* __restrict__ ws) {
  extern __shared__ __attribute__((aligned(16))) char lds[];
  const int tid = threadIdx.x, lane = tid & 63, wid = tid >> 6;
  const int bid = blockIdx.x;
  STAMP(ws, 0, 0);
  if (bid >= L1_NQ + L1_NKV + L1_NLN) {
    const int pb = bid - (L1_NQ + L1_NKV + L1_NLN);
    const int tl = (pb >> 3) * 512 + tid;
    const int t = pb * 512 + tid;
    u32x4 v[23];
#pragma unroll
    for (int r = 0; r < 3; ++r) { const int c = r * 8192 + tl; v[r] = ((const u32x4*)(x + (size_t)(pb & 7) * 256 * D_))[c < 20480 ? c : 0]; }
#pragma unroll
    for (int r = 0; r < 7; ++r) { const int c = r * 8192 + tl; v[3 + r] = ((const u32x4*)wq)[c < 51200 ? c : 0]; }
#pragma unroll
    for (int r = 0; r < 6; ++r) v[10 + r] = ((const u32x4*)w1)[r * 65536 + t];
#pragma unroll
    for (int r = 0; r < 6; ++r) v[16 + r] = ((const u32x4*)w2)[r * 65536 + t];
    v[22] = ((const u32x4*)wo)[t < 51200 ? t : 0];
#pragma unroll
    for (int r = 0; r < 23; ++r) asm volatile("" :: "v"(v[r]));
    return;
  }
  if (bid < L1_NQ) {
    const int mt = (bid & 7) * 4 + (bid >> 6), nt = (bid >> 3) & 7;
    f32x4 acc[5] = {};
    gemm80<true, false, 4>(acc, lds, x + (size_t)mt * 64 * D_, D_, wq + nt * 80, D_, 10, tid);
    u16* t = (u16*)lds;
    if (wid < 4) {
#pragma unroll
      for (int j = 0; j < 5; ++j)
#pragma unroll
        for (int r = 0; r < 4; ++r)
          t[(wid * 16 + (lane >> 4) * 4 + r) * 80 + j * 16 + (lane & 15)] = f2bf(acc[j][r] * QSCALE);
    }
    __syncthreads();
    u16* q = (u16*)(ws + WS_Q) + (size_t)mt * 64 * D_ + nt * 80;
    for (int i = tid; i < 640; i += 512) {
      const int row = i / 10, ch = i % 10;
      *(u32x4*)(q + (size_t)row * D_ + ch * 8) = *(const u32x4*)(lds + i * 16);
    }
    STAMP(ws, 0, 1);
  } else if (bid < L1_NQ + L1_NKV) {
    const int b = bid - L1_NQ;
    const int mt = (b & 7) * 4 + (b >> 3);
    const u16* xa = x + (size_t)mt * 64 * D_;
    const int uw = __builtin_amdgcn_readfirstlane(wid);
    const int wm = (wid >> 1) & 1, isv = wid & 1, h = lane >> 5;
    const int blk = (lane >> 4) & 1, qq = (lane & 15) >> 2, p = lane & 3;
    constexpr int KST = 14336, NBUF = 5;
    f32x16 acc = {};
    if (uw >= 4) {
      auto stage = [&](int kt, int buf) {
        char* l = lds + buf * KST;
        stage_kc<64, 256>(l, xa + kt * 64, D_, tid - 256);
#pragma unroll
        for (int i = 0; i < 2; ++i) {
          const int pc = i * 4 + uw - 4;
          if (pc < 6) {
            const int c = (pc % 3) * 64 + lane;
            glds16((pc < 3 ? wk : wv) + kt * 1280, (unsigned)(c < 160 ? c : 159) * 16u, l + 8192 + pc * 1024);
          }
        }
      };
#pragma unroll
      for (int i = 0; i < NBUF - 1; ++i) stage(i, i);
      int bn = NBUF - 1;
      for (int kt = 0; kt < 10; ++kt) {
        if (kt + NBUF - 1 <= 10) { if (uw < 6) wait_vm<4 * (NBUF - 2)>(); else wait_vm<3 * (NBUF - 2)>(); }
        else wait_vm<0>();
        __builtin_amdgcn_s_barrier();
        if (kt + NBUF - 1 < 10) stage(kt + NBUF - 1, bn);
        ++bn; if (bn == NBUF) bn = 0;
      }
    } else {
      int buf = 0;
      for (int kt = 0; kt < 10; ++kt) {
        raw_barrier();
        const char* la = lds + buf * KST;
        const char* lw = la + 8192 + isv * 3072;
#pragma unroll
        for (int ks = 0; ks < 4; ++ks) {
          const bf16x8 xf = frag32_kc(la, wm * 32, ks, lane);
          const char* a = lw + (16 * ks + 8 * h + qq) * 40 + (16 * blk + 4 * p) * 2;
          const bf16x8 wf = cat8(tr_read(a), tr_read(a + 4 * 40));
          if (isv) acc = mfma32(xf, wf, acc);
          else     acc = mfma32(wf, xf, acc);
        }
        ++buf; if (buf == NBUF) buf = 0;
      }
    }
    const int tile = mt * 2 + wm;
    char* img = ws + WS_KV + (size_t)(tile >> 3) * KV_CHUNK;
    const int tl = tile & 7;
    float* red = (float*)lds;
    __syncthreads();
    if (uw >= 4) {
    } else if (!isv) {
      u32x4 a; a[0] = pk_bf16(acc[0], acc[1]); a[1] = pk_bf16(acc[2], acc[3]);
      a[2] = pk_bf16(acc[4], acc[5]); a[3] = pk_bf16(acc[6], acc[7]);
      *(u32x4*)(img + tl * 1024 + lane * 16) = a;
      float n2 = 0.f;
#pragma unroll
      for (int i = 0; i < 4; ++i) { float lo = bf_lo(a[i]), hi = bf_hi(a[i]); n2 += lo * lo + hi * hi; }
      u32x2 bb;
      if (h == 0) {
        bb[0] = pk_bf16(acc[8], acc[9]); bb[1] = pk_bf16(acc[10], acc[11]);
#pragma unroll
        for (int i = 0; i < 2; ++i) { float lo = bf_lo(bb[i]), hi = bf_hi(bb[i]); n2 += lo * lo + hi * hi; }
      } else { bb[0] = 0x3f803f80u; bb[1] = 0x00003f80u; }
      *(u32x2*)(img + KV_KB_OFF + tl * 512 + lane * 8) = bb;
      n2 += __shfl_xor(n2, 32);
#pragma unroll
      for (int o = 16; o > 0; o >>= 1) n2 = fmaxf(n2, __shfl_xor(n2, o));
      if (lane == 0) red[wm] = n2;
    } else {
      const int d = lane & 31;
      if (d <= 20) {
#pragma unroll
        for (int s = 0; s < 2; ++s) {
          u32x4 a;
#pragma unroll
          for (int i = 0; i < 4; ++i) a[i] = d < 20 ? pk_bf16(acc[8 * s + 2 * i], acc[8 * s + 2 * i + 1]) : 0x3f803f80u;
          *(u32x4*)(img + KV_V_OFF + tl * 1344 + (s * 2 + h) * 336 + d * 16) = a;
        }
      }
    }
    __syncthreads();
    if (tid == 0) ((float*)(ws + WS_KN))[mt] = fmaxf(red[0], red[1]);
    if (b == 0 && tid < 16) ((unsigned*)(ws + WS_CNT))[tid * 32] = 0u;
    STAMP(ws, 0, 1);
  } else {
    const int b = bid - L1_NQ - L1_NKV;
    const int s0 = (b & 7) * 256 + (b >> 3) * 32 + wid * 4;
    unsigned gwv[5], xv[4][5];
#pragma unroll
    for (int t = 0; t < 4; ++t)
#pragma unroll
      for (int i = 0; i < 5; ++i) xv[t][i] = ((const unsigned*)(x + (size_t)(s0 + t) * D_))[lane + 64 * i];
#pragma unroll
    for (int i = 0; i < 5; ++i) gwv[i] = ((const unsigned*)gw)[lane + 64 * i];
    float sum[4], gs[4], vs[4];
#pragma unroll
    for (int t = 0; t < 4; ++t) {
      sum[t] = 0.f; gs[t] = 0.f;
#pragma unroll
      for (int i = 0; i < 5; ++i) {
        sum[t] += bf_lo(xv[t][i]) + bf_hi(xv[t][i]);
        gs[t] += bf_lo(xv[t][i]) * bf_lo(gwv[i]) + bf_hi(xv[t][i]) * bf_hi(gwv[i]);
      }
    }
#pragma unroll
    for (int o = 32; o > 0; o >>= 1)
#pragma unroll
      for (int t = 0; t < 4; ++t) { sum[t] += __shfl_xor(sum[t], o); gs[t] += __shfl_xor(gs[t], o); }
#pragma unroll
    for (int t = 0; t < 4; ++t) {
      sum[t] *= (1.f / D_); vs[t] = 0.f;
#pragma unroll
      for (int i = 0; i < 5; ++i) {
        const float a = bf_lo(xv[t][i]) - sum[t], c = bf_hi(xv[t][i]) - sum[t];
        vs[t] += a * a + c * c;
      }
    }
#pragma unroll
    for (int o = 32; o > 0; o >>= 1)
#pragma unroll
      for (int t = 0; t < 4; ++t) vs[t] += __shfl_xor(vs[t], o);
#pragma unroll
    for (int t = 0; t < 4; ++t) {
      const float rstd = 1.f / sqrtf(vs[t] * (1.f / D_) + 1e-5f);
      unsigned* hr = (unsigned*)(ws + WS_H + (size_t)(s0 + t) * D_ * 2);
#pragma unroll
      for (int i = 0; i < 5; ++i)
        hr[lane + 64 * i] = pk_bf16((bf_lo(xv[t][i]) - sum[t]) * rstd, (bf_hi(xv[t][i]) - sum[t]) * rstd);
      if (lane == 0) ((float*)(ws + WS_G))[s0 + t] = 1.f / (1.f + __expf(-gs[t]));
    }
    STAMP(ws, 0, 1);
  }
}

constexpr int L2_LDS = 4 * T80_STG;

DI float gelu_tanh(float u) {
  const float z = 0.7978845608028654f * (u + 0.044715f * u * u * u);
  const float e = __expf(2.f * z);
  const float t = 1.f - 2.f / (e + 1.f);
  return 0.5f * u * (1.f + t);
}

DI void attn_stage(char* lds, const char* kv, int chunk, int buf, int wid, int lane) {
  const char* g = kv + (size_t)chunk * KV_CHUNK;
  char* l = lds + buf * KV_CHUNK;
#pragma unroll
  for (int r = 0; r < 3; ++r) {
    const int p = r * 8 + wid;
    if (p < 23) glds16(g + p * 1024, (unsigned)lane * 16u, l + p * 1024);
  }
}

typedef __attribute__((address_space(1))) unsigned gu32;

__global__ __launch_bounds__(512, 4) void k_main(const u16* __restrict__ w1, const u16* __restrict__ b1,
                                                 const u16* __restrict__ w2, const u16* __restrict__ b2,
                                                 char* __restrict__ ws) {
  extern __shared__ __attribute__((aligned(16))) char lds[];
  const int tid = threadIdx.x, lane = tid & 63, wid = __builtin_amdgcn_readfirstlane(tid >> 6);
  const int bid = blockIdx.x;
  STAMP(ws, 1, 0);
  if (bid < 256) {
    __builtin_amdgcn_s_setprio(3);
    const int xcd = bid & 7, i = bid >> 3;
    gu32* cnt = (gu32*)(ws + WS_CNT);
    if (i < 30) {
      const int mt = i / 3, nt = xcd * 3 + i % 3;
      const u16* A = (const u16*)(ws + WS_H) + mt * 64;
      const u16* B = w1 + nt * 64;
      const int wm = (wid >> 1) & 1, wn = wid & 1;
      f32x16 acc = {};
      constexpr int NBUF = 4, NK = 32;
      if (wid >= 4) {
        Stager<1, 1, 8, 8, false> st;
        st.init(A, D_, B, F_, wid - 4, lane);
        st.run<NBUF, 16384>(lds, NK);
      } else {
        for (int kt = 0; kt < NK; ++kt) {
          raw_barrier();
          const char* la = lds + (kt & 3) * 16384;
          bf16x8 af[4], bfr[4];
#pragma unroll
          for (int ks = 0; ks < 4; ++ks) { af[ks] = frag32_ks64(la, wm * 32, ks, lane); bfr[ks] = frag32_ks64(la + 8192, wn * 32, ks, lane); }
#pragma unroll
          for (int ks = 0; ks < 4; ++ks) acc = mfma32(af[ks], bfr[ks], acc);
        }
      }
      raw_barrier();
      u16* t = (u16*)(lds + 16384);
      if (wid < 4) {
        const int h = lane >> 5, fl = wn * 32 + (lane & 31);
        const float bb = bf2f(b1[nt * 64 + fl]);
#pragma unroll
        for (int r = 0; r < 16; ++r) t[(wm * 32 + crow(r, h)) * 64 + fl] = f2bf(gelu_tanh(acc[r] + bb));
      }
      __syncthreads();
      {
        const int row = tid >> 3, ch = tid & 7;
        const u32x4 v = *(const u32x4*)(lds + 16384 + tid * 16);
        u16* dst = (u16*)(ws + WS_U) + (size_t)(mt * 64 + row) * F_ + nt * 64 + ch * 8;
        asm volatile("global_store_dwordx4 %0, %1, off sc1\n\ts_nop 1" :: "v"(dst), "v"(v) : "memory");
      }
      wait_vm<0>();
      __syncthreads();
      if (tid == 0) __hip_atomic_fetch_add(cnt + mt * 32, 1u, __ATOMIC_RELAXED, __HIP_MEMORY_SCOPE_AGENT);
    }
    STAMP(ws, 1, 1);
    if (wid == 0) {
      for (int spin = 0; spin < (1 << 22); ++spin) {
        const unsigned c = __hip_atomic_load(cnt + (lane < 10 ? lane : 0) * 32, __ATOMIC_RELAXED, __HIP_MEMORY_SCOPE_AGENT);
        if (__all(c >= 24u)) break;
        __builtin_amdgcn_s_sleep(2);
      }
    }
    __syncthreads();
    STAMP(ws, 1, 2);
    {
      const int mt = xcd * 4 + (i >> 3), nt = i & 7;
      f32x4 acc[5] = {};
      gemm80<false, true, 4, true>(acc, lds, w2 + mt * 64, S_, (const u16*)(ws + WS_U) + (size_t)nt * 80 * F_, F_, 24, tid);
      u16* t = (u16*)(lds + 20480);
      if (wid < 4) {
#pragma unroll
        for (int r = 0; r < 4; ++r) {
          const int row = wid * 16 + (lane >> 4) * 4 + r;
          const float bb = bf2f(b2[mt * 64 + row]);
#pragma unroll
          for (int j = 0; j < 5; ++j) t[row * 80 + j * 16 + (lane & 15)] = f2bf(acc[j][r] + bb);
        }
      }
      __syncthreads();
      u16* y = (u16*)(ws + WS_Y) + (size_t)mt * 64 * D_ + nt * 80;
      for (int c = tid; c < 640; c += 512) {
        const int row = c / 10, ch = c % 10;
        *(u32x4*)(y + (size_t)row * D_ + ch * 8) = *(const u32x4*)(lds + 20480 + c * 16);
      }
    }
    STAMP(ws, 1, 3);
    return;
  }
  const int ab = bid - 256;
  const int s = ((ab & 7) * 32 + (ab >> 3)) * 8 + wid;
  const int h = lane >> 5, hd = lane & 31;
  const char* kv = ws + WS_KV;
  float* red = (float*)(lds + 3 * KV_CHUNK);
  const u16* qr = (const u16*)(ws + WS_Q) + (size_t)s * D_ + hd * 20;
  const u32x2 qa = *(const u32x2*)(qr + 4 * h), qb = *(const u32x2*)(qr + 8 + 4 * h);
  const u32x2 qc = *(const u32x2*)(qr + 16);
  float kmax2 = ((const float*)(ws + WS_KN))[hd];
  attn_stage(lds, kv, 0, 0, wid, lane);
  attn_stage(lds, kv, 1, 1, wid, lane);
#pragma unroll
  for (int o = 16; o > 0; o >>= 1) kmax2 = fmaxf(kmax2, __shfl_xor(kmax2, o));
  float qn2 = 0.f;
#pragma unroll
  for (int i = 0; i < 2; ++i) {
    qn2 += bf_lo(qa[i]) * bf_lo(qa[i]) + bf_hi(qa[i]) * bf_hi(qa[i]);
    qn2 += bf_lo(qb[i]) * bf_lo(qb[i]) + bf_hi(qb[i]) * bf_hi(qb[i]);
    if (h == 0) qn2 += bf_lo(qc[i]) * bf_lo(qc[i]) + bf_hi(qc[i]) * bf_hi(qc[i]);
  }
  qn2 += __shfl_xor(qn2, 32);
  u32x4 b1u; b1u[0] = qa[0]; b1u[1] = qa[1]; b1u[2] = qb[0]; b1u[3] = qb[1];
  const bf16x8 B1 = __builtin_bit_cast(bf16x8, b1u);
  float mshift = sqrtf(qn2 * kmax2);
  const int ka_lane = lane * 16, kb_lane = KV_KB_OFF + lane * 8;
  const int v_lane = KV_V_OFF + h * 336 + (hd < 20 ? hd : 20) * 16;
  f32x16 O;
  for (int pass = 0; pass < 2; ++pass) {
    if (pass == 1) {
      s16x4 B2z; { u32x2 t = qc; if (h) { t[0] = 0; t[1] = 0; } B2z = __builtin_bit_cast(s16x4, t); }
      float mx = -3.0e38f;
      for (int c = 0; c < 8; ++c) {
        attn_stage(lds, kv, c, 0, wid, lane);
        wait_vm<0>(); raw_barrier();
        for (int t = 0; t < 8; ++t) {
          const bf16x8 A1 = *(const bf16x8*)(lds + t * 1024 + ka_lane);
          const s16x4 A2 = *(const s16x4*)(lds + t * 512 + kb_lane);
          f32x16 Sx = {};
          Sx = mfma32(A1, B1, Sx); Sx = mfma32k8(A2, B2z, Sx);
#pragma unroll
          for (int r = 0; r < 16; ++r) mx = fmaxf(mx, Sx[r]);
        }
        raw_barrier();
      }
      mx = fmaxf(mx, __shfl_xor(mx, 32));
      mshift = mx;
      attn_stage(lds, kv, 0, 0, wid, lane);
      attn_stage(lds, kv, 1, 1, wid, lane);
    }
    s16x4 B2;
    {
      const float m0 = bf2f(f2bf(mshift)), m1 = bf2f(f2bf(mshift - m0)), m2 = bf2f(f2bf(mshift - m0 - m1));
      u32x2 t = qc;
      if (h) { t[0] = pk_bf16(-m0, -m1); t[1] = pk_bf16(-m2, 0.f); }
      B2 = __builtin_bit_cast(s16x4, t);
    }
    STAMP(ws, 1, 1);
    O = f32x16{};
    f32x16 Sa, Sb;
    bf16x8 KA1; s16x4 KA2;
    auto tile = [&](const f32x16& Sc, f32x16& Sn, const char* vp, const char* k2a, const char* k2b) {
      const bf16x8 V0 = *(const bf16x8*)(vp), V1 = *(const bf16x8*)(vp + 672);
      u32x4 p0, p1;
      p0[0] = pk_bf16(__builtin_amdgcn_exp2f(Sc[0]), __builtin_amdgcn_exp2f(Sc[1]));
      p0[1] = pk_bf16(__builtin_amdgcn_exp2f(Sc[2]), __builtin_amdgcn_exp2f(Sc[3]));
      SB();
      Sn = mfma32(KA1, B1, f32x16{});
      SB();
      p0[2] = pk_bf16(__builtin_amdgcn_exp2f(Sc[4]), __builtin_amdgcn_exp2f(Sc[5]));
      p0[3] = pk_bf16(__builtin_amdgcn_exp2f(Sc[6]), __builtin_amdgcn_exp2f(Sc[7]));
      SB();
      Sn = mfma32k8(KA2, B2, Sn);
      KA1 = *(const bf16x8*)(k2a); KA2 = *(const s16x4*)(k2b);
      SB();
      p1[0] = pk_bf16(__builtin_amdgcn_exp2f(Sc[8]), __builtin_amdgcn_exp2f(Sc[9]));
      p1[1] = pk_bf16(__builtin_amdgcn_exp2f(Sc[10]), __builtin_amdgcn_exp2f(Sc[11]));
      SB();
      O = mfma32(V0, __builtin_bit_cast(bf16x8, p0), O);
      SB();
      p1[2] = pk_bf16(__builtin_amdgcn_exp2f(Sc[12]), __builtin_amdgcn_exp2f(Sc[13]));
      p1[3] = pk_bf16(__builtin_amdgcn_exp2f(Sc[14]), __builtin_amdgcn_exp2f(Sc[15]));
      SB();
      O = mfma32(V1, __builtin_bit_cast(bf16x8, p1), O);
      SB();
    };
    int bc = 0;
    for (int c = 0; c < 8; ++c) {
      wait_vm<0>(); raw_barrier();
      int bn = bc + 1; if (bn == 3) bn = 0;
      int b2n = bn + 1; if (b2n == 3) b2n = 0;
      if (c + 2 < 8) attn_stage(lds, kv, c + 2, b2n, wid, lane);
      const char* l = lds + bc * KV_CHUNK;
      const char* ln = lds + bn * KV_CHUNK;
      if (c == 0) {
        const bf16x8 A1 = *(const bf16x8*)(l + ka_lane);
        const s16x4 A2 = *(const s16x4*)(l + kb_lane);
        KA1 = *(const bf16x8*)(l + 1024 + ka_lane); KA2 = *(const s16x4*)(l + 512 + kb_lane);
        Sa = mfma32(A1, B1, f32x16{}); Sa = mfma32k8(A2, B2, Sa);
      }
#pragma unroll
      for (int t = 0; t < 8; t += 2) {
        const char* ka = t + 2 < 8 ? l + (t + 2) * 1024 : ln + (t - 6) * 1024;
        const char* kb = t + 2 < 8 ? l + (t + 2) * 512 : ln + (t - 6) * 512;
        tile(Sa, Sb, l + v_lane + t * 1344, ka + ka_lane, kb + kb_lane);
        tile(Sb, Sa, l + v_lane + (t + 1) * 1344, ka + 1024 + ka_lane, kb + 512 + kb_lane);
      }
      bc = bn;
    }
    STAMP(ws, 1, 2);
    const float lsum = __shfl(O[8], hd + 32);
    const bool bad = !(lsum >= 8.271806e-25f && lsum <= 3.0e38f);
    if (pass == 1) break;
    raw_barrier();
    if (tid == 0) red[0] = 0.f;
    __syncthreads();
    if (__any(bad) && lane == 0) red[0] = 1.f;
    __syncthreads();
    if (red[0] == 0.f) break;
    __syncthreads();
  }
  {
    const float lsum = __shfl(O[8], hd + 32);
    const float inv = 1.f / lsum;
    u16* orow = (u16*)(ws + WS_O) + (size_t)s * D_ + hd * 20 + 4 * h;
    u32x2 a, b;
    a[0] = pk_bf16(O[0] * inv, O[1] * inv); a[1] = pk_bf16(O[2] * inv, O[3] * inv);
    b[0] = pk_bf16(O[4] * inv, O[5] * inv); b[1] = pk_bf16(O[6] * inv, O[7] * inv);
    *(u32x2*)(orow) = a;
    *(u32x2*)(orow + 8) = b;
    if (h == 0) {
      u32x2 c; c[0] = pk_bf16(O[8] * inv, O[9] * inv); c[1] = pk_bf16(O[10] * inv, O[11] * inv);
      *(u32x2*)(orow + 16) = c;
    }
  }
  STAMP(ws, 1, 3);
}

constexpr int L3_LDS = 4 * T80_STG;
__global__ __launch_bounds__(512) void k_final(const u16* __restrict__ wo, const char* __restrict__ ws,
                                               u16* __restrict__ out) {
  extern __shared__ __attribute__((aligned(16))) char lds[];
  const int tid = threadIdx.x, lane = tid & 63, wid = tid >> 6;
  const int bid = blockIdx.x;
  STAMP(ws, 2, 0);
  const int mt = (bid & 7) * 4 + (bid >> 6), nt = (bid >> 3) & 7;
  const float* g = (const float*)(ws + WS_G) + mt * 64;
  const u16* y = (const u16*)(ws + WS_Y) + (size_t)mt * 64 * D_ + nt * 80;
  u32x4 yv[2]; float gv[2];
#pragma unroll
  for (int k = 0; k < 2; ++k) {
    const int i = (tid + 512 * k) < 640 ? tid + 512 * k : tid;
    yv[k] = *(const u32x4*)(y + (size_t)(i / 10) * D_ + (i % 10) * 8); gv[k] = g[i / 10];
  }
  f32x4 acc[5] = {};
  gemm80<true, false, 4>(acc, lds, (const u16*)(ws + WS_O) + (size_t)mt * 64 * D_, D_, wo + nt * 80, D_, 10, tid);
  STAMP(ws, 2, 1);
  float* t = (float*)lds;
  if (wid < 4) {
#pragma unroll
    for (int j = 0; j < 5; ++j)
#pragma unroll
      for (int r = 0; r < 4; ++r) t[(wid * 16 + (lane >> 4) * 4 + r) * 84 + j * 16 + (lane & 15)] = acc[j][r];
  }
  __syncthreads();
  u16* o = out + (size_t)mt * 64 * D_ + nt * 80;
#pragma unroll
  for (int k = 0; k < 2; ++k) {
    const int i = tid + 512 * k;
    if (i < 640) {
      const int row = i / 10, ch = i % 10;
      const float gg = gv[k];
      const f32x4 a0 = *(const f32x4*)(t + row * 84 + ch * 8), a1 = *(const f32x4*)(t + row * 84 + ch * 8 + 4);
      u32x4 r;
      r[0] = pk_bf16(gg * a0[0] + (1.f - gg) * bf_lo(yv[k][0]), gg * a0[1] + (1.f - gg) * bf_hi(yv[k][0]));
      r[1] = pk_bf16(gg * a0[2] + (1.f - gg) * bf_lo(yv[k][1]), gg * a0[3] + (1.f - gg) * bf_hi(yv[k][1]));
      r[2] = pk_bf16(gg * a1[0] + (1.f - gg) * bf_lo(yv[k][2]), gg * a1[1] + (1.f - gg) * bf_hi(yv[k][2]));
      r[3] = pk_bf16(gg * a1[2] + (1.f - gg) * bf_lo(yv[k][3]), gg * a1[3] + (1.f - gg) * bf_hi(yv[k][3]));
      *(u32x4*)(o + (size_t)row * D_ + ch * 8) = r;
    }
  }
  STAMP(ws, 2, 2);
}

extern "C" void kernel_launch(void* const* d_in, const int* in_sizes, int n_in, void* d_out, int out_size,
                              void* d_ws, size_t ws_size, hipStream_t stream) {
  const u16* x = (const u16*)d_in[0];
  const u16* wq = (const u16*)d_in[1];
  const u16* wk = (const u16*)d_in[2];
  const u16* wv = (const u16*)d_in[3];
  const u16* wo = (const u16*)d_in[4];
  const u16* w1 = (const u16*)d_in[6];
  const u16* b1 = (const u16*)d_in[7];
  const u16* w2 = (const u16*)d_in[8];
  const u16* b2 = (const u16*)d_in[9];
  const u16* gw = (const u16*)d_in[10];
  char* ws = (char*)d_ws;
  (void)hipFuncSetAttribute((const void*)k_proj, hipFuncAttributeMaxDynamicSharedMemorySize, L1_LDS);
  (void)hipFuncSetAttribute((const void*)k_main, hipFuncAttributeMaxDynamicSharedMemorySize, L2_LDS);
  (void)hipFuncSetAttribute((const void*)k_final, hipFuncAttributeMaxDynamicSharedMemorySize, L3_LDS);
  k_proj<<<L1_NQ + L1_NKV + L1_NLN + L1_NPF, 512, L1_LDS, stream>>>(x, wq, wk, wv, gw, w1, w2, wo, ws);
  k_main<<<512, 512, L2_LDS, stream>>>(w1, b1, w2, b2, ws);
  k_final<<<256, 512, L3_LDS, stream>>>(wo, ws, (u16*)d_out);
}
```

```cpp
#include <hip/hip_runtime.h>
#include <hip/hip_bf16.h>
#include <stdint.h>

typedef unsigned short u16;
typedef __attribute__((ext_vector_type(8))) short bf16x8;
typedef __attribute__((ext_vector_type(4))) short s16x4;
typedef __attribute__((ext_vector_type(16))) float f32x16;
typedef __attribute__((ext_vector_type(4))) float f32x4;
typedef __attribute__((ext_vector_type(4))) unsigned u32x4;
typedef __attribute__((ext_vector_type(2))) unsigned u32x2;
typedef __attribute__((ext_vector_type(2))) __bf16 bf16x2_t;
#define LDS_AS __attribute__((address_space(3)))
#define DI __device__ __forceinline__
#define SB() __builtin_amdgcn_sched_barrier(0)

constexpr int S_ = 2048, D_ = 640, F_ = 1536;
#define STAMP(ws, kid, slot) do {} while (0)

constexpr size_t WS_CNT = 0;
constexpr size_t WS_Q   = 8192;
constexpr size_t WS_H   = WS_Q + (size_t)S_ * D_ * 2;
constexpr size_t WS_O   = WS_H + (size_t)S_ * D_ * 2;
constexpr size_t WS_Y   = WS_O + (size_t)S_ * D_ * 2;
constexpr size_t WS_U   = WS_Y + (size_t)S_ * D_ * 2;
constexpr size_t WS_KV  = WS_U + (size_t)D_ * F_ * 2;
constexpr int    KV_CHUNK = 23552;
constexpr int    KV_KB_OFF = 8192, KV_V_OFF = 12288;
constexpr size_t WS_KN  = WS_KV + 8 * KV_CHUNK;
constexpr size_t WS_G   = WS_KN + 256;

template <bool SC1 = false>
DI void glds16(const void* gbase, unsigned voff, const void* lds_wave_base) {
  const unsigned dst = __builtin_amdgcn_readfirstlane((unsigned)(uintptr_t)lds_wave_base);
  const unsigned lo = __builtin_amdgcn_readfirstlane((unsigned)(uintptr_t)gbase);
  const unsigned hi = __builtin_amdgcn_readfirstlane((unsigned)((uintptr_t)gbase >> 32));
  const unsigned long long gb = ((unsigned long long)hi << 32) | lo;
  unsigned keep;
  if constexpr (SC1)
    asm volatile("s_mov_b32 %0, m0\n\ts_mov_b32 m0, %3\n\ts_nop 0\n\tglobal_load_lds_dwordx4 %1, %2 sc1\n\ts_mov_b32 m0, %0"
                 : "=&s"(keep) : "v"(voff), "s"(gb), "s"(dst) : "memory");
  else
    asm volatile("s_mov_b32 %0, m0\n\ts_mov_b32 m0, %3\n\ts_nop 0\n\tglobal_load_lds_dwordx4 %1, %2\n\ts_mov_b32 m0, %0"
                 : "=&s"(keep) : "v"(voff), "s"(gb), "s"(dst) : "memory");
}
DI unsigned pk_bf16(float a, float b) {
  bf16x2_t p; p[0] = (__bf16)a; p[1] = (__bf16)b; return __builtin_bit_cast(unsigned, p);
}
DI float bf_lo(unsigned u) { return __uint_as_float(u << 16); }
DI float bf_hi(unsigned u) { return __uint_as_float(u & 0xffff0000u); }
DI float bf2f(u16 v) { return __uint_as_float((unsigned)v << 16); }
DI u16 f2bf(float f) { return (u16)(pk_bf16(f, 0.f) & 0xffffu); }
DI f32x16 mfma32(bf16x8 a, bf16x8 b, f32x16 c) { return __builtin_amdgcn_mfma_f32_32x32x16_bf16(a, b, c, 0, 0, 0); }
DI f32x16 mfma32k8(s16x4 a, s16x4 b, f32x16 c) { return __builtin_amdgcn_mfma_f32_32x32x8bf16_1k(a, b, c, 0, 0, 0); }
DI f32x4 mfma16(bf16x8 a, bf16x8 b, f32x4 c) { return __builtin_amdgcn_mfma_f32_16x16x32_bf16(a, b, c, 0, 0, 0); }
DI s16x4 tr_read(const char* p) { return __builtin_amdgcn_ds_read_tr16_b64_v4i16((LDS_AS s16x4*)p); }
DI bf16x8 cat8(s16x4 lo, s16x4 hi) { return __builtin_shufflevector(lo, hi, 0, 1, 2, 3, 4, 5, 6, 7); }
template <int N> DI void wait_vm() { asm volatile("s_waitcnt vmcnt(%0)" :: "i"(N) : "memory"); }
DI void raw_barrier() {
  asm volatile("s_waitcnt lgkmcnt(0)" ::: "memory");
  __builtin_amdgcn_s_barrier();
  asm volatile("" ::: "memory");
}
DI int crow(int r, int h) { return (r & 3) + 8 * (r >> 2) + 4 * h; }

template <int R, int NT, bool SC1 = false> DI void stage_kc(char* lds, const u16* g, int ld, int tid) {
  constexpr int NCH = R * 8, N = (NCH + NT - 1) / NT;
#pragma unroll
  for (int i = 0; i < N; ++i) {
    const int idx = i * NT + tid;
    if ((i + 1) * NT <= NCH || (i * NT + __builtin_amdgcn_readfirstlane(tid & ~63)) < NCH) {
      const int row = idx >> 3, ch = (idx & 7) ^ ((row >> 1) & 7);
      glds16<SC1>(g, (unsigned)(row * ld + ch * 8) * 2u, lds + (i * NT + __builtin_amdgcn_readfirstlane(tid & ~63)) * 16);
    }
  }
}
template <int C> DI int swz_ks(int k) { return C == 64 ? ((((k >> 1) & 1) << 2) | (((k >> 3) & 1) << 1)) : 0; }
template <int C, int NT> DI void stage_ks(char* lds, const u16* g, int ld, int tid) {
  constexpr int CPR = C / 8, NCH = 64 * CPR, N = (NCH + NT - 1) / NT;
#pragma unroll
  for (int i = 0; i < N; ++i) {
    const int idx = i * NT + tid;
    if ((i + 1) * NT <= NCH || (i * NT + __builtin_amdgcn_readfirstlane(tid & ~63)) < NCH) {
      const int k = idx / CPR, ch = (idx % CPR) ^ swz_ks<C>(k);
      glds16(g, (unsigned)(k * ld + ch * 8) * 2u, lds + (i * NT + __builtin_amdgcn_readfirstlane(tid & ~63)) * 16);
    }
  }
}
template <int KIND> DI unsigned piece_voff(int p, int lane, int ld) {
  const int idx = p * 64 + lane;
  if constexpr (KIND == 0) { const int row = idx >> 3, ch = (idx & 7) ^ ((row >> 1) & 7); return (unsigned)(row * ld + ch * 8) * 2u; }
  else if constexpr (KIND == 1) { const int k = idx >> 3, ch = (idx & 7) ^ swz_ks<64>(k); return (unsigned)(k * ld + ch * 8) * 2u; }
  else { const int k = idx / 10, ch = idx % 10; return (unsigned)(k * ld + ch * 8) * 2u; }
}
template <int AK, int BKD, int NPA, int NPB, bool BSC1>
struct Stager {
  static constexpr int NP = NPA + NPB, BASE = NP / 4, EXTRA = NP % 4, NJ = BASE + (EXTRA ? 1 : 0);
  unsigned voff[NJ]; int p0, n; const u16* base[NJ]; int step[NJ];
  DI void init(const u16* A, int lda, const u16* B, int ldb, int w, int lane) {
    n = BASE + (w < EXTRA ? 1 : 0); p0 = w * BASE + (w < EXTRA ? w : EXTRA);
#pragma unroll
    for (int j = 0; j < NJ; ++j) {
      const int pj = p0 + j;
      const bool isa = pj < NPA;
      voff[j] = isa ? piece_voff<AK>(pj, lane, lda) : piece_voff<BKD>(pj - NPA, lane, ldb);
      base[j] = isa ? A : B;
      step[j] = isa ? (AK == 0 ? 64 : 64 * lda) : (BKD == 0 ? 64 : 64 * ldb);
    }
  }
  DI void stage(char* l, int kt) const {
#pragma unroll
    for (int j = 0; j < NJ; ++j)
      if (j < BASE || j < n) glds16<BSC1>(base[j] + (size_t)kt * step[j], voff[j], l + (p0 + j) * 1024);
  }
  template <int M> DI void wait() const {
    if (n == BASE) wait_vm<BASE * M>(); else wait_vm<(BASE + 1) * M>();
  }
  template <int NBUF, int STG> DI void run(char* lds, int nk) const {
#pragma unroll
    for (int i = 0; i < NBUF - 1; ++i) stage(lds + i * STG, i);
    int bn = NBUF - 1;
    for (int kt = 0; kt < nk; ++kt) {
      if (kt + NBUF - 1 <= nk) wait<NBUF - 2>(); else wait_vm<0>();
      __builtin_amdgcn_s_barrier();
      if (kt + NBUF - 1 < nk) stage(lds + bn * STG, kt + NBUF - 1);
      ++bn; if (bn == NBUF) bn = 0;
    }
  }
};

DI bf16x8 frag32_ks64(const char* lds, int c0, int ks, int lane) {
  const int h = lane >> 5, blk = (lane >> 4) & 1, q = (lane & 15) >> 2, p = lane & 3;
  const int k = 16 * ks + 8 * h + q, ch = ((c0 >> 3) + 2 * blk + (p >> 1)) ^ swz_ks<64>(k);
  const char* a = lds + k * 128 + ch * 16 + 8 * (p & 1);
  return cat8(tr_read(a), tr_read(a + 4 * 128));
}
DI bf16x8 frag32_kc(const char* lds, int row0, int ks, int lane) {
  const int row = row0 + (lane & 31), ch = (2 * ks + (lane >> 5)) ^ ((row >> 1) & 7);
  return *(const bf16x8*)(lds + row * 128 + ch * 16);
}
DI bf16x8 frag16_kc(const char* lds, int row0, int ks, int lane) {
  const int row = row0 + (lane & 15), ch = (4 * ks + (lane >> 4)) ^ ((row >> 1) & 7);
  return *(const bf16x8*)(lds + row * 128 + ch * 16);
}
template <int C> DI bf16x8 frag16_ks(const char* lds, int c0, int ks, int lane) {
  const int g = lane >> 4, q = (lane & 15) >> 2, p = lane & 3;
  const int k = 32 * ks + 8 * g + q, ch = ((c0 >> 3) + (p >> 1)) ^ swz_ks<C>(k);
  const char* a = lds + k * (2 * C) + ch * 16 + 8 * (p & 1);
  return cat8(tr_read(a), tr_read(a + 4 * (2 * C)));
}

constexpr int T80_STG = 64 * 128 + 80 * 128;
template <bool AKC, bool BKC, int NBUF, bool BSC1 = false>
DI void gemm80(f32x4 (&acc)[5], char* lds, const u16* A, int lda, const u16* B, int ldb, int nk, int tid) {
  const int lane = tid & 63, wid = __builtin_amdgcn_readfirstlane(tid >> 6);
  if (wid >= 4) {
    Stager<AKC ? 0 : 1, BKC ? 0 : 2, 8, 10, BSC1> st;
    st.init(A, lda, B, ldb, wid - 4, lane);
    st.template run<NBUF, T80_STG>(lds, nk);
  } else {
    int buf = 0;
    for (int kt = 0; kt < nk; ++kt) {
      raw_barrier();
      bf16x8 af[2], bfr[2][5];
      const char* la = lds + buf * T80_STG;
      const char* lb = la + 8192;
#pragma unroll
      for (int ks = 0; ks < 2; ++ks) {
        if constexpr (AKC) af[ks] = frag16_kc(la, wid * 16, ks, lane); else af[ks] = frag16_ks<64>(la, wid * 16, ks, lane);
#pragma unroll
        for (int j = 0; j < 5; ++j) {
          if constexpr (BKC) bfr[ks][j] = frag16_kc(lb, j * 16, ks, lane); else bfr[ks][j] = frag16_ks<80>(lb, j * 16, ks, lane);
        }
      }
#pragma unroll
      for (int ks = 0; ks < 2; ++ks)
#pragma unroll
        for (int j = 0; j < 5; ++j) acc[j] = mfma16(af[ks], bfr[ks][j], acc[j]);
      ++buf; if (buf == NBUF) buf = 0;
    }
  }
  raw_barrier();
}

constexpr int L1_NQ = 256, L1_NKV = 32, L1_NLN = 64, L1_NPF = 128;
constexpr int L1_LDS = 4 * T80_STG;
constexpr float QSCALE = 0.22360679774997896f * 1.4426950408889634f;

__global__ __launch_bounds__(512) void k_proj(const u16* __restrict__ x, const u16* __restrict__ wq,
                                              const u16* __restrict__ wk, const u16* __restrict__ wv,
                                              const u16* __restrict__ gw, const u16* __restrict__ w1,
                                              char* __restrict__ ws) {
  extern __shared__ __attribute__((aligned(16))) char lds[];
  const int tid = threadIdx.x, lane = tid & 63, wid = tid >> 6;
  const int bid = blockIdx.x;
  STAMP(ws, 0, 0);
  if (bid >= L1_NQ + L1_NKV + L1_NLN) {
    const int pb = bid - (L1_NQ + L1_NKV + L1_NLN);
    const int tl = (pb >> 3) * 512 + tid;
    const int t = pb * 512 + tid;
    u32x4 v[16];
#pragma unroll
    for (int r = 0; r < 3; ++r) { const int c = r * 8192 + tl; v[r] = ((const u32x4*)(x + (size_t)(pb & 7) * 256 * D_))[c < 20480 ? c : 0]; }
#pragma unroll
    for (int r = 0; r < 7; ++r) { const int c = r * 8192 + tl; v[3 + r] = ((const u32x4*)wq)[c < 51200 ? c : 0]; }
#pragma unroll
    for (int r = 0; r < 6; ++r) v[10 + r] = ((const u32x4*)w1)[r * 65536 + t];
#pragma unroll
    for (int r = 0; r < 16; ++r) asm volatile("" :: "v"(v[r]));
    return;
  }
  if (bid < L1_NQ) {
    const int mt = (bid & 7) * 4 + (bid >> 6), nt = (bid >> 3) & 7;
    f32x4 acc[5] = {};
    gemm80<true, false, 4>(acc, lds, x + (size_t)mt * 64 * D_, D_, wq + nt * 80, D_, 10, tid);
    u16* t = (u16*)lds;
    if (wid < 4) {
#pragma unroll
      for (int j = 0; j < 5; ++j)
#pragma unroll
        for (int r = 0; r < 4; ++r)
          t[(wid * 16 + (lane >> 4) * 4 + r) * 80 + j * 16 + (lane & 15)] = f2bf(acc[j][r] * QSCALE);
    }
    __syncthreads();
    u16* q = (u16*)(ws + WS_Q) + (size_t)mt * 64 * D_ + nt * 80;
    for (int i = tid; i < 640; i += 512) {
      const int row = i / 10, ch = i % 10;
      *(u32x4*)(q + (size_t)row * D_ + ch * 8) = *(const u32x4*)(lds + i * 16);
    }
    STAMP(ws, 0, 1);
  } else if (bid < L1_NQ + L1_NKV) {
    const int b = bid - L1_NQ;
    const int mt = (b & 7) * 4 + (b >> 3);
    const u16* xa = x + (size_t)mt * 64 * D_;
    const int uw = __builtin_amdgcn_readfirstlane(wid);
    const int wm = (wid >> 1) & 1, isv = wid & 1, h = lane >> 5;
    const int blk = (lane >> 4) & 1, qq = (lane & 15) >> 2, p = lane & 3;
    constexpr int KST = 14336, NBUF = 5;
    f32x16 acc = {};
    if (uw >= 4) {
      auto stage = [&](int kt, int buf) {
        char* l = lds + buf * KST;
        stage_kc<64, 256>(l, xa + kt * 64, D_, tid - 256);
#pragma unroll
        for (int i = 0; i < 2; ++i) {
          const int pc = i * 4 + uw - 4;
          if (pc < 6) {
            const int c = (pc % 3) * 64 + lane;
            glds16((pc < 3 ? wk : wv) + kt * 1280, (unsigned)(c < 160 ? c : 159) * 16u, l + 8192 + pc * 1024);
          }
        }
      };
#pragma unroll
      for (int i = 0; i < NBUF - 1; ++i) stage(i, i);
      int bn = NBUF - 1;
      for (int kt = 0; kt < 10; ++kt) {
        if (kt + NBUF - 1 <= 10) { if (uw < 6) wait_vm<4 * (NBUF - 2)>(); else wait_vm<3 * (NBUF - 2)>(); }
        else wait_vm<0>();
        __builtin_amdgcn_s_barrier();
        if (kt + NBUF - 1 < 10) stage(kt + NBUF - 1, bn);
        ++bn; if (bn == NBUF) bn = 0;
      }
    } else {
      int buf = 0;
      for (int kt = 0; kt < 10; ++kt) {
        raw_barrier();
        const char* la = lds + buf * KST;
        const char* lw = la + 8192 + isv * 3072;
#pragma unroll
        for (int ks = 0; ks < 4; ++ks) {
          const bf16x8 xf = frag32_kc(la, wm * 32, ks, lane);
          const char* a = lw + (16 * ks + 8 * h + qq) * 40 + (16 * blk + 4 * p) * 2;
          const bf16x8 wf = cat8(tr_read(a), tr_read(a + 4 * 40));
          if (isv) acc = mfma32(xf, wf, acc);
          else     acc = mfma32(wf, xf, acc);
        }
        ++buf; if (buf == NBUF) buf = 0;
      }
    }
    const int tile = mt * 2 + wm;
    char* img = ws + WS_KV + (size_t)(tile >> 3) * KV_CHUNK;
    const int tl = tile & 7;
    float* red = (float*)lds;
    __syncthreads();
    if (uw >= 4) {
    } else if (!isv) {
      u32x4 a; a[0] = pk_bf16(acc[0], acc[1]); a[1] = pk_bf16(acc[2], acc[3]);
      a[2] = pk_bf16(acc[4], acc[5]); a[3] = pk_bf16(acc[6], acc[7]);
      *(u32x4*)(img + tl * 1024 + lane * 16) = a;
      float n2 = 0.f;
#pragma unroll
      for (int i = 0; i < 4; ++i) { float lo = bf_lo(a[i]), hi = bf_hi(a[i]); n2 += lo * lo + hi * hi; }
      u32x2 bb;
      if (h == 0) {
        bb[0] = pk_bf16(acc[8], acc[9]); bb[1] = pk_bf16(acc[10], acc[11]);
#pragma unroll
        for (int i = 0; i < 2; ++i) { float lo = bf_lo(bb[i]), hi = bf_hi(bb[i]); n2 += lo * lo + hi * hi; }
      } else { bb[0] = 0x3f803f80u; bb[1] = 0x00003f80u; }
      *(u32x2*)(img + KV_KB_OFF + tl * 512 + lane * 8) = bb;
      n2 += __shfl_xor(n2, 32);
#pragma unroll
      for (int o = 16; o > 0; o >>= 1) n2 = fmaxf(n2, __shfl_xor(n2, o));
      if (lane == 0) red[wm] = n2;
    } else {
      const int d = lane & 31;
      if (d <= 20) {
#pragma unroll
        for (int s = 0; s < 2; ++s) {
          u32x4 a;
#pragma unroll
          for (int i = 0; i < 4; ++i) a[i] = d < 20 ? pk_bf16(acc[8 * s + 2 * i], acc[8 * s + 2 * i + 1]) : 0x3f803f80u;
          *(u32x4*)(img + KV_V_OFF + tl * 1344 + (s * 2 + h) * 336 + d * 16) = a;
        }
      }
    }
    __syncthreads();
    if (tid == 0) ((float*)(ws + WS_KN))[mt] = fmaxf(red[0], red[1]);
    if (b == 0 && tid < 48) ((unsigned*)(ws + WS_CNT))[tid * 32] = 0u;
    STAMP(ws, 0, 1);
  } else {
    const int b = bid - L1_NQ - L1_NKV;
    const int s0 = (b & 7) * 256 + (b >> 3) * 32 + wid * 4;
    unsigned gwv[5], xv[4][5];
#pragma unroll
    for (int t = 0; t < 4; ++t)
#pragma unroll
      for (int i = 0; i < 5; ++i) xv[t][i] = ((const unsigned*)(x + (size_t)(s0 + t) * D_))[lane + 64 * i];
#pragma unroll
    for (int i = 0; i < 5; ++i) gwv[i] = ((const unsigned*)gw)[lane + 64 * i];
    float sum[4], gs[4], vs[4];
#pragma unroll
    for (int t = 0; t < 4; ++t) {
      sum[t] = 0.f; gs[t] = 0.f;
#pragma unroll
      for (int i = 0; i < 5; ++i) {
        sum[t] += bf_lo(xv[t][i]) + bf_hi(xv[t][i]);
        gs[t] += bf_lo(xv[t][i]) * bf_lo(gwv[i]) + bf_hi(xv[t][i]) * bf_hi(gwv[i]);
      }
    }
#pragma unroll
    for (int o = 32; o > 0; o >>= 1)
#pragma unroll
      for (int t = 0; t < 4; ++t) { sum[t] += __shfl_xor(sum[t], o); gs[t] += __shfl_xor(gs[t], o); }
#pragma unroll
    for (int t = 0; t < 4; ++t) {
      sum[t] *= (1.f / D_); vs[t] = 0.f;
#pragma unroll
      for (int i = 0; i < 5; ++i) {
        const float a = bf_lo(xv[t][i]) - sum[t], c = bf_hi(xv[t][i]) - sum[t];
        vs[t] += a * a + c * c;
      }
    }
#pragma unroll
    for (int o = 32; o > 0; o >>= 1)
#pragma unroll
      for (int t = 0; t < 4; ++t) vs[t] += __shfl_xor(vs[t], o);
#pragma unroll
    for (int t = 0; t < 4; ++t) {
      const float rstd = 1.f / sqrtf(vs[t] * (1.f / D_) + 1e-5f);
      unsigned* hr = (unsigned*)(ws + WS_H + (size_t)(s0 + t) * D_ * 2);
#pragma unroll
      for (int i = 0; i < 5; ++i)
        hr[lane + 64 * i] = pk_bf16((bf_lo(xv[t][i]) - sum[t]) * rstd, (bf_hi(xv[t][i]) - sum[t]) * rstd);
      if (lane == 0) ((float*)(ws + WS_G))[s0 + t] = 1.f / (1.f + __expf(-gs[t]));
    }
    STAMP(ws, 0, 1);
  }
}

constexpr int L2_LDS = 4 * T80_STG;

DI float gelu_tanh(float u) {
  const float z = 0.7978845608028654f * (u + 0.044715f * u * u * u);
  const float e = __expf(2.f * z);
  const float t = 1.f - 2.f / (e + 1.f);
  return 0.5f * u * (1.f + t);
}

DI void attn_stage(char* lds, const char* kv, int chunk, int buf, int wid, int lane) {
  const char* g = kv + (size_t)chunk * KV_CHUNK;
  char* l = lds + buf * KV_CHUNK;
#pragma unroll
  for (int r = 0; r < 3; ++r) {
    const int p = r * 8 + wid;
    if (p < 23) glds16(g + p * 1024, (unsigned)lane * 16u, l + p * 1024);
  }
}

typedef __attribute__((address_space(1))) unsigned gu32;

__global__ __launch_bounds__(512, 4) void k_main(const u16* __restrict__ w1, const u16* __restrict__ b1,
                                                 const u16* __restrict__ w2, const u16* __restrict__ b2,
                                                 const u16* __restrict__ wo, char* __restrict__ ws,
                                                 u16* __restrict__ out) {
  extern __shared__ __attribute__((aligned(16))) char lds[];
  const int tid = threadIdx.x, lane = tid & 63, wid = __builtin_amdgcn_readfirstlane(tid >> 6);
  const int bid = blockIdx.x ^ 256;
  STAMP(ws, 1, 0);
  if (bid < 256) {
    __builtin_amdgcn_s_setprio(3);
    const int xcd = bid & 7, i = bid >> 3;
    gu32* cnt = (gu32*)(ws + WS_CNT);
    __builtin_amdgcn_s_sleep(25);
    if (wid < 4) {
      u32x4 v[7];
#pragma unroll
      for (int r = 0; r < 6; ++r) v[r] = ((const u32x4*)w2)[bid * 1536 + r * 256 + tid];
      v[6] = ((const u32x4*)wo)[bid * 200 + (tid < 200 ? tid : 0)];
#pragma unroll
      for (int r = 0; r < 7; ++r) asm volatile("" :: "v"(v[r]));
    }
    if (i < 30) {
      const int mt = i / 3, nt = xcd * 3 + i % 3;
      const u16* A = (const u16*)(ws + WS_H) + mt * 64;
      const u16* B = w1 + nt * 64;
      const int wm = (wid >> 1) & 1, wn = wid & 1;
      f32x16 acc = {};
      constexpr int NBUF = 4, NK = 32;
      if (wid >= 4) {
        Stager<1, 1, 8, 8, false> st;
        st.init(A, D_, B, F_, wid - 4, lane);
        st.run<NBUF, 16384>(lds, NK);
      } else {
        for (int kt = 0; kt < NK; ++kt) {
          raw_barrier();
          const char* la = lds + (kt & 3) * 16384;
          bf16x8 af[4], bfr[4];
#pragma unroll
          for (int ks = 0; ks < 4; ++ks) { af[ks] = frag32_ks64(la, wm * 32, ks, lane); bfr[ks] = frag32_ks64(la + 8192, wn * 32, ks, lane); }
#pragma unroll
          for (int ks = 0; ks < 4; ++ks) acc = mfma32(af[ks], bfr[ks], acc);
        }
      }
      raw_barrier();
      u16* t = (u16*)(lds + 16384);
      if (wid < 4) {
        const int h = lane >> 5, fl = wn * 32 + (lane & 31);
        const float bb = bf2f(b1[nt * 64 + fl]);
#pragma unroll
        for (int r = 0; r < 16; ++r) t[(wm * 32 + crow(r, h)) * 64 + fl] = f2bf(gelu_tanh(acc[r] + bb));
      }
      __syncthreads();
      {
        const int row = tid >> 3, ch = tid & 7;
        const u32x4 v = *(const u32x4*)(lds + 16384 + tid * 16);
        u16* dst = (u16*)(ws + WS_U) + (size_t)(mt * 64 + row) * F_ + nt * 64 + ch * 8;
        asm volatile("global_store_dwordx4 %0, %1, off sc1\n\ts_nop 1" :: "v"(dst), "v"(v) : "memory");
      }
      wait_vm<0>();
      __syncthreads();
      if (tid == 0) __hip_atomic_fetch_add(cnt + mt * 32, 1u, __ATOMIC_RELAXED, __HIP_MEMORY_SCOPE_AGENT);
    }
    STAMP(ws, 1, 1);
    if (wid == 0) {
      for (int spin = 0; spin < (1 << 22); ++spin) {
        const unsigned c = __hip_atomic_load(cnt + (lane < 10 ? lane : 0) * 32, __ATOMIC_RELAXED, __HIP_MEMORY_SCOPE_AGENT);
        if (__all(c >= 24u)) break;
        __builtin_amdgcn_s_sleep(2);
      }
    }
    __syncthreads();
    STAMP(ws, 1, 2);
    {
      const int mt = xcd * 4 + (i >> 3), nt = i & 7;
      f32x4 acc[5] = {};
      gemm80<false, true, 4, true>(acc, lds, w2 + mt * 64, S_, (const u16*)(ws + WS_U) + (size_t)nt * 80 * F_, F_, 24, tid);
      if (wid == 0) {
        for (int spin = 0; spin < (1 << 22); ++spin) {
          const unsigned c = __hip_atomic_load(cnt + (16 + mt) * 32, __ATOMIC_RELAXED, __HIP_MEMORY_SCOPE_AGENT);
          if (c >= 8u) break;
          __builtin_amdgcn_s_sleep(4);
        }
      }
      float gv[4], bv[4];
      if (wid < 4) {
#pragma unroll
        for (int r = 0; r < 4; ++r) {
          const int row = mt * 64 + wid * 16 + (lane >> 4) * 4 + r;
          gv[r] = ((const float*)(ws + WS_G))[row]; bv[r] = bf2f(b2[row]);
        }
      }
      __syncthreads();
      STAMP(ws, 1, 4);
      f32x4 acca[5] = {};
      gemm80<true, false, 4, true>(acca, lds, (const u16*)(ws + WS_O) + (size_t)mt * 64 * D_, D_, wo + nt * 80, D_, 10, tid);
      u16* t = (u16*)lds;
      if (wid < 4) {
#pragma unroll
        for (int r = 0; r < 4; ++r) {
          const int row = wid * 16 + (lane >> 4) * 4 + r;
#pragma unroll
          for (int j = 0; j < 5; ++j)
            t[row * 80 + j * 16 + (lane & 15)] = f2bf(gv[r] * acca[j][r] + (1.f - gv[r]) * (acc[j][r] + bv[r]));
        }
      }
      __syncthreads();
      u16* o = out + (size_t)mt * 64 * D_ + nt * 80;
      for (int c = tid; c < 640; c += 512) {
        const int row = c / 10, ch = c % 10;
        *(u32x4*)(o + (size_t)row * D_ + ch * 8) = *(const u32x4*)(lds + c * 16);
      }
    }
    STAMP(ws, 1, 3);
    return;
  }
  const int ab = bid - 256;
  const int s = ((ab & 7) * 32 + (ab >> 3)) * 8 + wid;
  const int h = lane >> 5, hd = lane & 31;
  const char* kv = ws + WS_KV;
  float* red = (float*)(lds + 3 * KV_CHUNK);
  if (tid == 0) red[0] = 0.f;
  const u16* qr = (const u16*)(ws + WS_Q) + (size_t)s * D_ + hd * 20;
  const u32x2 qa = *(const u32x2*)(qr + 4 * h), qb = *(const u32x2*)(qr + 8 + 4 * h);
  const u32x2 qc = *(const u32x2*)(qr + 16);
  float kmax2 = ((const float*)(ws + WS_KN))[hd];
  attn_stage(lds, kv, 0, 0, wid, lane);
  attn_stage(lds, kv, 1, 1, wid, lane);
#pragma unroll
  for (int o = 16; o > 0; o >>= 1) kmax2 = fmaxf(kmax2, __shfl_xor(kmax2, o));
  float qn2 = 0.f;
#pragma unroll
  for (int i = 0; i < 2; ++i) {
    qn2 += bf_lo(qa[i]) * bf_lo(qa[i]) + bf_hi(qa[i]) * bf_hi(qa[i]);
    qn2 += bf_lo(qb[i]) * bf_lo(qb[i]) + bf_hi(qb[i]) * bf_hi(qb[i]);
    if (h == 0) qn2 += bf_lo(qc[i]) * bf_lo(qc[i]) + bf_hi(qc[i]) * bf_hi(qc[i]);
  }
  qn2 += __shfl_xor(qn2, 32);
  u32x4 b1u; b1u[0] = qa[0]; b1u[1] = qa[1]; b1u[2] = qb[0]; b1u[3] = qb[1];
  const bf16x8 B1 = __builtin_bit_cast(bf16x8, b1u);
  float mshift = sqrtf(qn2 * kmax2);
  const int ka_lane = lane * 16, kb_lane = KV_KB_OFF + lane * 8;
  const int v_lane = KV_V_OFF + h * 336 + (hd < 20 ? hd : 20) * 16;
  f32x16 O;
  for (int pass = 0; pass < 2; ++pass) {
    if (pass == 1) {
      s16x4 B2z; { u32x2 t = qc; if (h) { t[0] = 0; t[1] = 0; } B2z = __builtin_bit_cast(s16x4, t); }
      float mx = -3.0e38f;
      for (int c = 0; c < 8; ++c) {
        attn_stage(lds, kv, c, 0, wid, lane);
        wait_vm<0>(); raw_barrier();
        for (int t = 0; t < 8; ++t) {
          const bf16x8 A1 = *(const bf16x8*)(lds + t * 1024 + ka_lane);
          const s16x4 A2 = *(const s16x4*)(lds + t * 512 + kb_lane);
          f32x16 Sx = {};
          Sx = mfma32(A1, B1, Sx); Sx = mfma32k8(A2, B2z, Sx);
#pragma unroll
          for (int r = 0; r < 16; ++r) mx = fmaxf(mx, Sx[r]);
        }
        raw_barrier();
      }
      mx = fmaxf(mx, __shfl_xor(mx, 32));
      mshift = mx;
      attn_stage(lds, kv, 0, 0, wid, lane);
      attn_stage(lds, kv, 1, 1, wid, lane);
    }
    s16x4 B2;
    {
      const float m0 = bf2f(f2bf(mshift)), m1 = bf2f(f2bf(mshift - m0)), m2 = bf2f(f2bf(mshift - m0 - m1));
      u32x2 t = qc;
      if (h) { t[0] = pk_bf16(-m0, -m1); t[1] = pk_bf16(-m2, 0.f); }
      B2 = __builtin_bit_cast(s16x4, t);
    }
    STAMP(ws, 1, 1);
    O = f32x16{};
    f32x16 Sa, Sb;
    bf16x8 KA1; s16x4 KA2;
    auto tile = [&](const f32x16& Sc, f32x16& Sn, const char* vp, const char* k2a, const char* k2b) {
      const bf16x8 V0 = *(const bf16x8*)(vp), V1 = *(const bf16x8*)(vp + 672);
      u32x4 p0, p1;
      p0[0] = pk_bf16(__builtin_amdgcn_exp2f(Sc[0]), __builtin_amdgcn_exp2f(Sc[1]));
      p0[1] = pk_bf16(__builtin_amdgcn_exp2f(Sc[2]), __builtin_amdgcn_exp2f(Sc[3]));
      SB();
      Sn = mfma32(KA1, B1, f32x16{});
      SB();
      p0[2] = pk_bf16(__builtin_amdgcn_exp2f(Sc[4]), __builtin_amdgcn_exp2f(Sc[5]));
      p0[3] = pk_bf16(__builtin_amdgcn_exp2f(Sc[6]), __builtin_amdgcn_exp2f(Sc[7]));
      SB();
      Sn = mfma32k8(KA2, B2, Sn);
      KA1 = *(const bf16x8*)(k2a); KA2 = *(const s16x4*)(k2b);
      SB();
      p1[0] = pk_bf16(__builtin_amdgcn_exp2f(Sc[8]), __builtin_amdgcn_exp2f(Sc[9]));
      p1[1] = pk_bf16(__builtin_amdgcn_exp2f(Sc[10]), __builtin_amdgcn_exp2f(Sc[11]));
      SB();
      O = mfma32(V0, __builtin_bit_cast(bf16x8, p0), O);
      SB();
      p1[2] = pk_bf16(__builtin_amdgcn_exp2f(Sc[12]), __builtin_amdgcn_exp2f(Sc[13]));
      p1[3] = pk_bf16(__builtin_amdgcn_exp2f(Sc[14]), __builtin_amdgcn_exp2f(Sc[15]));
      SB();
      O = mfma32(V1, __builtin_bit_cast(bf16x8, p1), O);
      SB();
    };
    int bc = 0;
    for (int c = 0; c < 8; ++c) {
      wait_vm<0>(); raw_barrier();
      int bn = bc + 1; if (bn == 3) bn = 0;
      int b2n = bn + 1; if (b2n == 3) b2n = 0;
      if (c + 2 < 8) attn_stage(lds, kv, c + 2, b2n, wid, lane);
      const char* l = lds + bc * KV_CHUNK;
      const char* ln = lds + bn * KV_CHUNK;
      if (c == 0) {
        const bf16x8 A1 = *(const bf16x8*)(l + ka_lane);
        const s16x4 A2 = *(const s16x4*)(l + kb_lane);
        KA1 = *(const bf16x8*)(l + 1024 + ka_lane); KA2 = *(const s16x4*)(l + 512 + kb_lane);
        Sa = mfma32(A1, B1, f32x16{}); Sa = mfma32k8(A2, B2, Sa);
      }
#pragma unroll
      for (int t = 0; t < 8; t += 2) {
        const char* ka = t + 2 < 8 ? l + (t + 2) * 1024 : ln + (t - 6) * 1024;
        const char* kb = t + 2 < 8 ? l + (t + 2) * 512 : ln + (t - 6) * 512;
        tile(Sa, Sb, l + v_lane + t * 1344, ka + ka_lane, kb + kb_lane);
        tile(Sb, Sa, l + v_lane + (t + 1) * 1344, ka + 1024 + ka_lane, kb + 512 + kb_lane);
      }
      bc = bn;
    }
    STAMP(ws, 1, 2);
    const float lsum = __shfl(O[8], hd + 32);
    const bool bad = !(lsum >= 8.271806e-25f && lsum <= 3.0e38f);
    if (pass == 1) break;
    if (__any(bad) && lane == 0) red[0] = 1.f;
    __syncthreads();
    if (red[0] == 0.f) break;
  }
  {
    const float lsum = __shfl(O[8], hd + 32);
    const float inv = 1.f / lsum;
    u16* orow = (u16*)(ws + WS_O) + (size_t)s * D_ + hd * 20 + 4 * h;
    u32x2 a, b;
    a[0] = pk_bf16(O[0] * inv, O[1] * inv); a[1] = pk_bf16(O[2] * inv, O[3] * inv);
    b[0] = pk_bf16(O[4] * inv, O[5] * inv); b[1] = pk_bf16(O[6] * inv, O[7] * inv);
    typedef __attribute__((address_space(1))) unsigned long long gu64;
    __hip_atomic_store((gu64*)(orow), __builtin_bit_cast(unsigned long long, a), __ATOMIC_RELAXED, __HIP_MEMORY_SCOPE_AGENT);
    __hip_atomic_store((gu64*)(orow + 8), __builtin_bit_cast(unsigned long long, b), __ATOMIC_RELAXED, __HIP_MEMORY_SCOPE_AGENT);
    if (h == 0) {
      u32x2 c; c[0] = pk_bf16(O[8] * inv, O[9] * inv); c[1] = pk_bf16(O[10] * inv, O[11] * inv);
      __hip_atomic_store((gu64*)(orow + 16), __builtin_bit_cast(unsigned long long, c), __ATOMIC_RELAXED, __HIP_MEMORY_SCOPE_AGENT);
    }
    wait_vm<0>();
    __syncthreads();
    if (tid == 0) __hip_atomic_fetch_add((gu32*)(ws + WS_CNT) + (16 + (s >> 6)) * 32, 1u, __ATOMIC_RELAXED, __HIP_MEMORY_SCOPE_AGENT);
  }
  STAMP(ws, 1, 3);
}

extern "C" void kernel_launch(void* const* d_in, const int* in_sizes, int n_in, void* d_out, int out_size,
                              void* d_ws, size_t ws_size, hipStream_t stream) {
  const u16* x = (const u16*)d_in[0];
  const u16* wq = (const u16*)d_in[1];
  const u16* wk = (const u16*)d_in[2];
  const u16* wv = (const u16*)d_in[3];
  const u16* wo = (const u16*)d_in[4];
  const u16* w1 = (const u16*)d_in[6];
  const u16* b1 = (const u16*)d_in[7];
  const u16* w2 = (const u16*)d_in[8];
  const u16* b2 = (const u16*)d_in[9];
  const u16* gw = (const u16*)d_in[10];
  char* ws = (char*)d_ws;
  (void)hipFuncSetAttribute((const void*)k_proj, hipFuncAttributeMaxDynamicSharedMemorySize, L1_LDS);
  (void)hipFuncSetAttribute((const void*)k_main, hipFuncAttributeMaxDynamicSharedMemorySize, L2_LDS);
  k_proj<<<L1_NQ + L1_NKV + L1_NLN + L1_NPF, 512, L1_LDS, stream>>>(x, wq, wk, wv, gw, w1, ws);
  k_main<<<512, 512, L2_LDS, stream>>>(w1, b1, w2, b2, wo, ws, (u16*)d_out);
}
```

```cpp
#include <hip/hip_runtime.h>
#include <hip/hip_bf16.h>
#include <stdint.h>

typedef unsigned short u16;
typedef __attribute__((ext_vector_type(8))) short bf16x8;
typedef __attribute__((ext_vector_type(4))) short s16x4;
typedef __attribute__((ext_vector_type(16))) float f32x16;
typedef __attribute__((ext_vector_type(4))) float f32x4;
typedef __attribute__((ext_vector_type(4))) unsigned u32x4;
typedef __attribute__((ext_vector_type(2))) unsigned u32x2;
typedef __attribute__((ext_vector_type(2))) __bf16 bf16x2_t;
#define LDS_AS __attribute__((address_space(3)))
#define DI __device__ __forceinline__
#define SB() __builtin_amdgcn_sched_barrier(0)

constexpr int S_ = 2048, D_ = 640, F_ = 1536;

constexpr size_t WS_CNT = 0;
constexpr size_t WS_Q   = 8192;
constexpr size_t WS_H   = WS_Q + (size_t)S_ * D_ * 2;
constexpr size_t WS_O   = WS_H + (size_t)S_ * D_ * 2;
constexpr size_t WS_Y   = WS_O + (size_t)S_ * D_ * 2;
constexpr size_t WS_U   = WS_Y + (size_t)S_ * D_ * 2;
constexpr size_t WS_KV  = WS_U + (size_t)D_ * F_ * 2;
constexpr int    KV_CHUNK = 23552;
constexpr int    KV_KB_OFF = 8192, KV_V_OFF = 12288;
constexpr size_t WS_KN  = WS_KV + 8 * KV_CHUNK;
constexpr size_t WS_G   = WS_KN + 256;

template <bool SC1 = false>
DI void glds16(const void* gbase, unsigned voff, const void* lds_wave_base) {
  const unsigned dst = __builtin_amdgcn_readfirstlane((unsigned)(uintptr_t)lds_wave_base);
  const unsigned lo = __builtin_amdgcn_readfirstlane((unsigned)(uintptr_t)gbase);
  const unsigned hi = __builtin_amdgcn_readfirstlane((unsigned)((uintptr_t)gbase >> 32));
  const unsigned long long gb = ((unsigned long long)hi << 32) | lo;
  unsigned keep;
  if constexpr (SC1)
    asm volatile("s_mov_b32 %0, m0\n\ts_mov_b32 m0, %3\n\ts_nop 0\n\tglobal_load_lds_dwordx4 %1, %2 sc1\n\ts_mov_b32 m0, %0"
                 : "=&s"(keep) : "v"(voff), "s"(gb), "s"(dst) : "memory");
  else
    asm volatile("s_mov_b32 %0, m0\n\ts_mov_b32 m0, %3\n\ts_nop 0\n\tglobal_load_lds_dwordx4 %1, %2\n\ts_mov_b32 m0, %0"
                 : "=&s"(keep) : "v"(voff), "s"(gb), "s"(dst) : "memory");
}
DI unsigned pk_bf16(float a, float b) {
  bf16x2_t p; p[0] = (__bf16)a; p[1] = (__bf16)b; return __builtin_bit_cast(unsigned, p);
}
DI float bf_lo(unsigned u) { return __uint_as_float(u << 16); }
DI float bf_hi(unsigned u) { return __uint_as_float(u & 0xffff0000u); }
DI float bf2f(u16 v) { return __uint_as_float((unsigned)v << 16); }
DI u16 f2bf(float f) { return (u16)(pk_bf16(f, 0.f) & 0xffffu); }
DI f32x16 mfma32(bf16x8 a, bf16x8 b, f32x16 c) { return __builtin_amdgcn_mfma_f32_32x32x16_bf16(a, b, c, 0, 0, 0); }
DI f32x16 mfma32k8(s16x4 a, s16x4 b, f32x16 c) { return __builtin_amdgcn_mfma_f32_32x32x8bf16_1k(a, b, c, 0, 0, 0); }
DI f32x4 mfma16(bf16x8 a, bf16x8 b, f32x4 c) { return __builtin_amdgcn_mfma_f32_16x16x32_bf16(a, b, c, 0, 0, 0); }
DI s16x4 tr_read(const char* p) { return __builtin_amdgcn_ds_read_tr16_b64_v4i16((LDS_AS s16x4*)p); }
DI bf16x8 cat8(s16x4 lo, s16x4 hi) { return __builtin_shufflevector(lo, hi, 0, 1, 2, 3, 4, 5, 6, 7); }
template <int N> DI void wait_vm() { asm volatile("s_waitcnt vmcnt(%0)" :: "i"(N) : "memory"); }
DI void raw_barrier() {
  asm volatile("s_waitcnt lgkmcnt(0)" ::: "memory");
  __builtin_amdgcn_s_barrier();
  asm volatile("" ::: "memory");
}
DI int crow(int r, int h) { return (r & 3) + 8 * (r >> 2) + 4 * h; }

template <int R, int NT, bool SC1 = false> DI void stage_kc(char* lds, const u16* g, int ld, int tid) {
  constexpr int NCH = R * 8, N = (NCH + NT - 1) / NT;
#pragma unroll
  for (int i = 0; i < N; ++i) {
    const int idx = i * NT + tid;
    if ((i + 1) * NT <= NCH || (i * NT + __builtin_amdgcn_readfirstlane(tid & ~63)) < NCH) {
      const int row = idx >> 3, ch = (idx & 7) ^ ((row >> 1) & 7);
      glds16<SC1>(g, (unsigned)(row * ld + ch * 8) * 2u, lds + (i * NT + __builtin_amdgcn_readfirstlane(tid & ~63)) * 16);
    }
  }
}
template <int C> DI int swz_ks(int k) { return C == 64 ? ((((k >> 1) & 1) << 2) | (((k >> 3) & 1) << 1)) : 0; }
template <int C, int NT> DI void stage_ks(char* lds, const u16* g, int ld, int tid) {
  constexpr int CPR = C / 8, NCH = 64 * CPR, N = (NCH + NT - 1) / NT;
#pragma unroll
  for (int i = 0; i < N; ++i) {
    const int idx = i * NT + tid;
    if ((i + 1) * NT <= NCH || (i * NT + __builtin_amdgcn_readfirstlane(tid & ~63)) < NCH) {
      const int k = idx / CPR, ch = (idx % CPR) ^ swz_ks<C>(k);
      glds16(g, (unsigned)(k * ld + ch * 8) * 2u, lds + (i * NT + __builtin_amdgcn_readfirstlane(tid & ~63)) * 16);
    }
  }
}
template <int KIND> DI unsigned piece_voff(int p, int lane, int ld) {
  const int idx = p * 64 + lane;
  if constexpr (KIND == 0) { const int row = idx >> 3, ch = (idx & 7) ^ ((row >> 1) & 7); return (unsigned)(row * ld + ch * 8) * 2u; }
  else if constexpr (KIND == 1) { const int k = idx >> 3, ch = (idx & 7) ^ swz_ks<64>(k); return (unsigned)(k * ld + ch * 8) * 2u; }
  else { const int k = idx / 10, ch = idx % 10; return (unsigned)(k * ld + ch * 8) * 2u; }
}
template <int AK, int BKD, int NPA, int NPB, bool BSC1>
struct Stager {
  static constexpr int NP = NPA + NPB, BASE = NP / 4, EXTRA = NP % 4, NJ = BASE + (EXTRA ? 1 : 0);
  unsigned voff[NJ]; int p0, n; const u16* base[NJ]; int step[NJ];
  DI void init(const u16* A, int lda, const u16* B, int ldb, int w, int lane) {
    n = BASE + (w < EXTRA ? 1 : 0); p0 = w * BASE + (w < EXTRA ? w : EXTRA);
#pragma unroll
    for (int j = 0; j < NJ; ++j) {
      const int pj = p0 + j;
      const bool isa = pj < NPA;
      voff[j] = isa ? piece_voff<AK>(pj, lane, lda) : piece_voff<BKD>(pj - NPA, lane, ldb);
      base[j] = isa ? A : B;
      step[j] = isa ? (AK == 0 ? 64 : 64 * lda) : (BKD == 0 ? 64 : 64 * ldb);
    }
  }
  DI void stage(char* l, int kt) const {
#pragma unroll
    for (int j = 0; j < NJ; ++j)
      if (j < BASE || j < n) glds16<BSC1>(base[j] + (size_t)kt * step[j], voff[j], l + (p0 + j) * 1024);
  }
  template <int M> DI void wait() const {
    if (n == BASE) wait_vm<BASE * M>(); else wait_vm<(BASE + 1) * M>();
  }
  template <int NBUF, int STG> DI void run(char* lds, int nk) const {
#pragma unroll
    for (int i = 0; i < NBUF - 1; ++i) stage(lds + i * STG, i);
    int bn = NBUF - 1;
    for (int kt = 0; kt < nk; ++kt) {
      if (kt + NBUF - 1 <= nk) wait<NBUF - 2>(); else wait_vm<0>();
      __builtin_amdgcn_s_barrier();
      if (kt + NBUF - 1 < nk) stage(lds + bn * STG, kt + NBUF - 1);
      ++bn; if (bn == NBUF) bn = 0;
    }
  }
};

DI bf16x8 frag32_ks64(const char* lds, int c0, int ks, int lane) {
  const int h = lane >> 5, blk = (lane >> 4) & 1, q = (lane & 15) >> 2, p = lane & 3;
  const int k = 16 * ks + 8 * h + q, ch = ((c0 >> 3) + 2 * blk + (p >> 1)) ^ swz_ks<64>(k);
  const char* a = lds + k * 128 + ch * 16 + 8 * (p & 1);
  return cat8(tr_read(a), tr_read(a + 4 * 128));
}
DI bf16x8 frag32_kc(const char* lds, int row0, int ks, int lane) {
  const int row = row0 + (lane & 31), ch = (2 * ks + (lane >> 5)) ^ ((row >> 1) & 7);
  return *(const bf16x8*)(lds + row * 128 + ch * 16);
}
DI bf16x8 frag16_kc(const char* lds, int row0, int ks, int lane) {
  const int row = row0 + (lane & 15), ch = (4 * ks + (lane >> 4)) ^ ((row >> 1) & 7);
  return *(const bf16x8*)(lds + row * 128 + ch * 16);
}
template <int C> DI bf16x8 frag16_ks(const char* lds, int c0, int ks, int lane) {
  const int g = lane >> 4, q = (lane & 15) >> 2, p = lane & 3;
  const int k = 32 * ks + 8 * g + q, ch = ((c0 >> 3) + (p >> 1)) ^ swz_ks<C>(k);
  const char* a = lds + k * (2 * C) + ch * 16 + 8 * (p & 1);
  return cat8(tr_read(a), tr_read(a + 4 * (2 * C)));
}

constexpr int T80_STG = 64 * 128 + 80 * 128;
template <bool AKC, bool BKC, int NBUF, bool BSC1 = false>
DI void gemm80(f32x4 (&acc)[5], char* lds, const u16* A, int lda, const u16* B, int ldb, int nk, int tid) {
  const int lane = tid & 63, wid = __builtin_amdgcn_readfirstlane(tid >> 6);
  if (wid >= 4) {
    Stager<AKC ? 0 : 1, BKC ? 0 : 2, 8, 10, BSC1> st;
    st.init(A, lda, B, ldb, wid - 4, lane);
    st.template run<NBUF, T80_STG>(lds, nk);
  } else {
    int buf = 0;
    for (int kt = 0; kt < nk; ++kt) {
      raw_barrier();
      bf16x8 af[2], bfr[2][5];
      const char* la = lds + buf * T80_STG;
      const char* lb = la + 8192;
#pragma unroll
      for (int ks = 0; ks < 2; ++ks) {
        if constexpr (AKC) af[ks] = frag16_kc(la, wid * 16, ks, lane); else af[ks] = frag16_ks<64>(la, wid * 16, ks, lane);
#pragma unroll
        for (int j = 0; j < 5; ++j) {
          if constexpr (BKC) bfr[ks][j] = frag16_kc(lb, j * 16, ks, lane); else bfr[ks][j] = frag16_ks<80>(lb, j * 16, ks, lane);
        }
      }
#pragma unroll
      for (int ks = 0; ks < 2; ++ks)
#pragma unroll
        for (int j = 0; j < 5; ++j) {
          acc[j] = mfma16(af[ks], bfr[ks][j], acc[j]);
        }
      ++buf; if (buf == NBUF) buf = 0;
    }
  }
  raw_barrier();
}

constexpr int L1_NQ = 256, L1_NKV = 32, L1_NLN = 64, L1_NPF = 128;
constexpr int L1_LDS = 4 * T80_STG;
constexpr float QSCALE = 0.22360679774997896f * 1.4426950408889634f;

__global__ __launch_bounds__(512) void k_proj(const u16* __restrict__ x, const u16* __restrict__ wq,
                                              const u16* __restrict__ wk, const u16* __restrict__ wv,
                                              const u16* __restrict__ gw, const u16* __restrict__ w1,
                                              char* __restrict__ ws) {
  extern __shared__ __attribute__((aligned(16))) char lds[];
  const int tid = threadIdx.x, lane = tid & 63, wid = tid >> 6;
  const int bid = blockIdx.x;
  if (bid >= L1_NQ + L1_NKV + L1_NLN) {
    const int pb = bid - (L1_NQ + L1_NKV + L1_NLN);
    const int tl = (pb >> 3) * 512 + tid;
    const int t = pb * 512 + tid;
    u32x4 v[17];
    v[16] = *(tl < 1600 ? (const u32x4*)wk + tl : (const u32x4*)wv + (tl < 3200 ? tl - 1600 : 0));
#pragma unroll
    for (int r = 0; r < 3; ++r) { const int c = r * 8192 + tl; v[r] = ((const u32x4*)(x + (size_t)(pb & 7) * 256 * D_))[c < 20480 ? c : 0]; }
#pragma unroll
    for (int r = 0; r < 7; ++r) { const int c = r * 8192 + tl; v[3 + r] = ((const u32x4*)wq)[c < 51200 ? c : 0]; }
#pragma unroll
    for (int r = 0; r < 6; ++r) v[10 + r] = ((const u32x4*)w1)[r * 65536 + t];
#pragma unroll
    for (int r = 0; r < 17; ++r) asm volatile("" :: "v"(v[r]));
    return;
  }
  if (bid < L1_NQ) {
    const int mt = (bid & 7) * 4 + (bid >> 6), nt = (bid >> 3) & 7;
    f32x4 acc[5] = {};
    gemm80<true, false, 4>(acc, lds, x + (size_t)mt * 64 * D_, D_, wq + nt * 80, D_, 10, tid);
    u16* t = (u16*)lds;
    if (wid < 4) {
#pragma unroll
      for (int j = 0; j < 5; ++j)
#pragma unroll
        for (int r = 0; r < 4; ++r)
          t[(wid * 16 + (lane >> 4) * 4 + r) * 80 + j * 16 + (lane & 15)] = f2bf(acc[j][r] * QSCALE);
    }
    __syncthreads();
    u16* q = (u16*)(ws + WS_Q) + (size_t)mt * 64 * D_ + nt * 80;
    for (int i = tid; i < 640; i += 512) {
      const int row = i / 10, ch = i % 10;
      *(u32x4*)(q + (size_t)row * D_ + ch * 8) = *(const u32x4*)(lds + i * 16);
    }
  } else if (bid < L1_NQ + L1_NKV) {
    const int b = bid - L1_NQ;
    const int mt = (b & 7) * 4 + (b >> 3);
    const u16* xa = x + (size_t)mt * 64 * D_;
    const int uw = __builtin_amdgcn_readfirstlane(wid);
    const int wm = (uw >> 1) & 1, isv = uw & 1, h = lane >> 5;
    const int blk = (lane >> 4) & 1, qq = (lane & 15) >> 2, p = lane & 3;
    constexpr int KST = 14336, NBUF = 5;
    f32x16 acc = {};
    if (uw >= 4) {
      auto stage = [&](int kt, int buf) {
        char* l = lds + buf * KST;
        stage_kc<64, 256>(l, xa + kt * 64, D_, tid - 256);
#pragma unroll
        for (int i = 0; i < 2; ++i) {
          const int pc = i * 4 + uw - 4;
          if (pc < 6) {
            const int c = (pc % 3) * 64 + lane;
            glds16((pc < 3 ? wk : wv) + kt * 1280, (unsigned)(c < 160 ? c : 159) * 16u, l + 8192 + pc * 1024);
          }
        }
      };
#pragma unroll
      for (int i = 0; i < NBUF - 1; ++i) stage(i, i);
      int bn = NBUF - 1;
      for (int kt = 0; kt < 10; ++kt) {
        if (kt + NBUF - 1 <= 10) { if (uw < 6) wait_vm<4 * (NBUF - 2)>(); else wait_vm<3 * (NBUF - 2)>(); }
        else wait_vm<0>();
        __builtin_amdgcn_s_barrier();
        if (kt + NBUF - 1 < 10) stage(kt + NBUF - 1, bn);
        ++bn; if (bn == NBUF) bn = 0;
      }
    } else {
      int buf = 0;
      for (int kt = 0; kt < 10; ++kt) {
        raw_barrier();
        const char* la = lds + buf * KST;
        const char* lw = la + 8192 + isv * 3072;
        bf16x8 xf[4], wf[4];
#pragma unroll
        for (int ks = 0; ks < 4; ++ks) {
          xf[ks] = frag32_kc(la, wm * 32, ks, lane);
          const char* a = lw + (16 * ks + 8 * h + qq) * 40 + (16 * blk + 4 * p) * 2;
          wf[ks] = cat8(tr_read(a), tr_read(a + 4 * 40));
        }
        if (isv) {
#pragma unroll
          for (int ks = 0; ks < 4; ++ks) acc = mfma32(xf[ks], wf[ks], acc);
        } else {
#pragma unroll
          for (int ks = 0; ks < 4; ++ks) acc = mfma32(wf[ks], xf[ks], acc);
        }
        ++buf; if (buf == NBUF) buf = 0;
      }
    }
    const int tile = mt * 2 + wm;
    char* img = ws + WS_KV + (size_t)(tile >> 3) * KV_CHUNK;
    const int tl = tile & 7;
    float* red = (float*)lds;
    __syncthreads();
    if (uw >= 4) {
    } else if (!isv) {
      u32x4 a; a[0] = pk_bf16(acc[0], acc[1]); a[1] = pk_bf16(acc[2], acc[3]);
      a[2] = pk_bf16(acc[4], acc[5]); a[3] = pk_bf16(acc[6], acc[7]);
      *(u32x4*)(img + tl * 1024 + lane * 16) = a;
      float n2 = 0.f;
#pragma unroll
      for (int i = 0; i < 4; ++i) { float lo = bf_lo(a[i]), hi = bf_hi(a[i]); n2 += lo * lo + hi * hi; }
      u32x2 bb;
      if (h == 0) {
        bb[0] = pk_bf16(acc[8], acc[9]); bb[1] = pk_bf16(acc[10], acc[11]);
#pragma unroll
        for (int i = 0; i < 2; ++i) { float lo = bf_lo(bb[i]), hi = bf_hi(bb[i]); n2 += lo * lo + hi * hi; }
      } else { bb[0] = 0x3f803f80u; bb[1] = 0x00003f80u; }
      *(u32x2*)(img + KV_KB_OFF + tl * 512 + lane * 8) = bb;
      n2 += __shfl_xor(n2, 32);
#pragma unroll
      for (int o = 16; o > 0; o >>= 1) n2 = fmaxf(n2, __shfl_xor(n2, o));
      if (lane == 0) red[wm] = n2;
    } else {
      const int d = lane & 31;
      if (d <= 21) {
#pragma unroll
        for (int s = 0; s < 2; ++s) {
          u32x4 a;
#pragma unroll
          for (int i = 0; i < 4; ++i) a[i] = d < 20 ? pk_bf16(acc[8 * s + 2 * i], acc[8 * s + 2 * i + 1]) : (d == 20 ? 0x3f803f80u : 0u);
          *(u32x4*)(img + KV_V_OFF + tl * 1408 + (s * 2 + h) * 352 + d * 16) = a;
        }
      }
    }
    __syncthreads();
    if (tid == 0) ((float*)(ws + WS_KN))[mt] = fmaxf(red[0], red[1]);
    if (b == 0 && tid < 48) ((unsigned*)(ws + WS_CNT))[tid * 32] = 0u;
  } else {
    const int b = bid - L1_NQ - L1_NKV;
    const int s0 = (b & 7) * 256 + (b >> 3) * 32 + wid * 4;
    unsigned gwv[5], xv[4][5];
#pragma unroll
    for (int t = 0; t < 4; ++t)
#pragma unroll
      for (int i = 0; i < 5; ++i) xv[t][i] = ((const unsigned*)(x + (size_t)(s0 + t) * D_))[lane + 64 * i];
#pragma unroll
    for (int i = 0; i < 5; ++i) gwv[i] = ((const unsigned*)gw)[lane + 64 * i];
    float sum[4], gs[4], vs[4];
#pragma unroll
    for (int t = 0; t < 4; ++t) {
      sum[t] = 0.f; gs[t] = 0.f;
#pragma unroll
      for (int i = 0; i < 5; ++i) {
        sum[t] += bf_lo(xv[t][i]) + bf_hi(xv[t][i]);
        gs[t] += bf_lo(xv[t][i]) * bf_lo(gwv[i]) + bf_hi(xv[t][i]) * bf_hi(gwv[i]);
      }
    }
#pragma unroll
    for (int o = 32; o > 0; o >>= 1)
#pragma unroll
      for (int t = 0; t < 4; ++t) { sum[t] += __shfl_xor(sum[t], o); gs[t] += __shfl_xor(gs[t], o); }
#pragma unroll
    for (int t = 0; t < 4; ++t) {
      sum[t] *= (1.f / D_); vs[t] = 0.f;
#pragma unroll
      for (int i = 0; i < 5; ++i) {
        const float a = bf_lo(xv[t][i]) - sum[t], c = bf_hi(xv[t][i]) - sum[t];
        vs[t] += a * a + c * c;
      }
    }
#pragma unroll
    for (int o = 32; o > 0; o >>= 1)
#pragma unroll
      for (int t = 0; t < 4; ++t) vs[t] += __shfl_xor(vs[t], o);
#pragma unroll
    for (int t = 0; t < 4; ++t) {
      const float rstd = 1.f / sqrtf(vs[t] * (1.f / D_) + 1e-5f);
      unsigned* hr = (unsigned*)(ws + WS_H + (size_t)(s0 + t) * D_ * 2);
#pragma unroll
      for (int i = 0; i < 5; ++i)
        hr[lane + 64 * i] = pk_bf16((bf_lo(xv[t][i]) - sum[t]) * rstd, (bf_hi(xv[t][i]) - sum[t]) * rstd);
      if (lane == 0) ((float*)(ws + WS_G))[s0 + t] = 1.f / (1.f + __expf(-gs[t]));
    }
  }
}

constexpr int L2_LDS = 4 * T80_STG;

DI float gelu_tanh(float u) {
  const float z = 0.7978845608028654f * (u + 0.044715f * u * u * u);
  const float e = __expf(2.f * z);
  const float t = 1.f - 2.f / (e + 1.f);
  return 0.5f * u * (1.f + t);
}

DI void attn_stage(char* lds, const char* kv, int chunk, int buf, int wid, int lane) {
  const char* g = kv + (size_t)chunk * KV_CHUNK;
  char* l = lds + buf * KV_CHUNK;
#pragma unroll
  for (int r = 0; r < 3; ++r) {
    const int p = r * 8 + wid;
    if (p < 23) glds16(g + p * 1024, (unsigned)lane * 16u, l + p * 1024);
  }
}

typedef __attribute__((address_space(1))) unsigned gu32;

__global__ __launch_bounds__(512, 4) void k_main(const u16* __restrict__ w1, const u16* __restrict__ b1,
                                                 const u16* __restrict__ w2, const u16* __restrict__ b2,
                                                 const u16* __restrict__ wo, char* __restrict__ ws,
                                                 u16* __restrict__ out) {
  extern __shared__ __attribute__((aligned(16))) char lds[];
  const int tid = threadIdx.x, lane = tid & 63, wid = __builtin_amdgcn_readfirstlane(tid >> 6);
  const int bid = blockIdx.x ^ 256;
  if (bid < 256) {
    __builtin_amdgcn_s_setprio(3);
    const int xcd = bid & 7, i = bid >> 3;
    gu32* cnt = (gu32*)(ws + WS_CNT);
    __builtin_amdgcn_s_sleep(25);
    if (wid < 4) {
      u32x4 v[7];
#pragma unroll
      for (int r = 0; r < 6; ++r) v[r] = ((const u32x4*)w2)[bid * 1536 + r * 256 + tid];
      v[6] = ((const u32x4*)wo)[bid * 200 + (tid < 200 ? tid : 0)];
#pragma unroll
      for (int r = 0; r < 7; ++r) asm volatile("" :: "v"(v[r]));
    }
    if (i < 30) {
      const int mt = i / 3, nt = xcd * 3 + i % 3;
      const u16* A = (const u16*)(ws + WS_H) + mt * 64;
      const u16* B = w1 + nt * 64;
      const int wm = (wid >> 1) & 1, wn = wid & 1;
      f32x16 acc = {};
      constexpr int NBUF = 4, NK = 32;
      if (wid >= 4) {
        Stager<1, 1, 8, 8, false> st;
        st.init(A, D_, B, F_, wid - 4, lane);
        st.run<NBUF, 16384>(lds, NK);
      } else {
        for (int kt = 0; kt < NK; ++kt) {
          raw_barrier();
          const char* la = lds + (kt & 3) * 16384;
          bf16x8 af[4], bfr[4];
#pragma unroll
          for (int ks = 0; ks < 4; ++ks) { af[ks] = frag32_ks64(la, wm * 32, ks, lane); bfr[ks] = frag32_ks64(la + 8192, wn * 32, ks, lane); }
#pragma unroll
          for (int ks = 0; ks < 4; ++ks) {
            acc = mfma32(af[ks], bfr[ks], acc);
          }
        }
      }
      raw_barrier();
      u16* t = (u16*)(lds + 16384);
      if (wid < 4) {
        const int h = lane >> 5, fl = wn * 32 + (lane & 31);
        const float bb = bf2f(b1[nt * 64 + fl]);
#pragma unroll
        for (int r = 0; r < 16; ++r) t[(wm * 32 + crow(r, h)) * 64 + fl] = f2bf(gelu_tanh(acc[r] + bb));
      }
      __syncthreads();
      {
        const int row = tid >> 3, ch = tid & 7;
        const u32x4 v = *(const u32x4*)(lds + 16384 + tid * 16);
        u16* dst = (u16*)(ws + WS_U) + (size_t)(mt * 64 + row) * F_ + nt * 64 + ch * 8;
        asm volatile("global_store_dwordx4 %0, %1, off sc1\n\ts_nop 1" :: "v"(dst), "v"(v) : "memory");
      }
      wait_vm<0>();
      __syncthreads();
      if (tid == 0) __hip_atomic_fetch_add(cnt + mt * 32, 1u, __ATOMIC_RELAXED, __HIP_MEMORY_SCOPE_AGENT);
    }
    if (wid == 0) {
      for (int spin = 0; spin < (1 << 22); ++spin) {
        const unsigned c = __hip_atomic_load(cnt + (lane < 10 ? lane : 0) * 32, __ATOMIC_RELAXED, __HIP_MEMORY_SCOPE_AGENT);
        if (__all(c >= 24u)) break;
        __builtin_amdgcn_s_sleep(2);
      }
    }
    __syncthreads();
    {
      const int mt = xcd * 4 + (i >> 3), nt = i & 7;
      f32x4 acc[5] = {};
      gemm80<false, true, 4, true>(acc, lds, w2 + mt * 64, S_, (const u16*)(ws + WS_U) + (size_t)nt * 80 * F_, F_, 24, tid);
      if (wid == 0) {
        for (int spin = 0; spin < (1 << 22); ++spin) {
          const unsigned c = __hip_atomic_load(cnt + (16 + mt) * 32, __ATOMIC_RELAXED, __HIP_MEMORY_SCOPE_AGENT);
          if (c >= 8u) break;
          __builtin_amdgcn_s_sleep(4);
        }
      }
      float gv[4], bv[4];
      if (wid < 4) {
#pragma unroll
        for (int r = 0; r < 4; ++r) {
          const int row = mt * 64 + wid * 16 + (lane >> 4) * 4 + r;
          gv[r] = ((const float*)(ws + WS_G))[row]; bv[r] = bf2f(b2[row]);
        }
      }
      __syncthreads();
      f32x4 acca[5] = {};
      gemm80<true, false, 4, true>(acca, lds, (const u16*)(ws + WS_O) + (size_t)mt * 64 * D_, D_, wo + nt * 80, D_, 10, tid);
      u16* t = (u16*)lds;
      if (wid < 4) {
#pragma unroll
        for (int r = 0; r < 4; ++r) {
          const int row = wid * 16 + (lane >> 4) * 4 + r;
#pragma unroll
          for (int j = 0; j < 5; ++j)
            t[row * 80 + j * 16 + (lane & 15)] = f2bf(gv[r] * acca[j][r] + (1.f - gv[r]) * (acc[j][r] + bv[r]));
        }
      }
      __syncthreads();
      u16* o = out + (size_t)mt * 64 * D_ + nt * 80;
      for (int c = tid; c < 640; c += 512) {
        const int row = c / 10, ch = c % 10;
        *(u32x4*)(o + (size_t)row * D_ + ch * 8) = *(const u32x4*)(lds + c * 16);
      }
    }
    return;
  }
  const int ab = bid - 256;
  const int s = ((ab & 7) * 32 + (ab >> 3)) * 8 + wid;
  const int h = lane >> 5, hd = lane & 31;
  const char* kv = ws + WS_KV;
  float* red = (float*)(lds + 3 * KV_CHUNK);
  if (tid == 0) red[0] = 0.f;
  const u16* qr = (const u16*)(ws + WS_Q) + (size_t)s * D_ + hd * 20;
  const u32x2 qa = *(const u32x2*)(qr + 4 * h), qb = *(const u32x2*)(qr + 8 + 4 * h);
  const u32x2 qc = *(const u32x2*)(qr + 16);
  float kmax2 = ((const float*)(ws + WS_KN))[hd];
  attn_stage(lds, kv, 0, 0, wid, lane);
  attn_stage(lds, kv, 1, 1, wid, lane);
#pragma unroll
  for (int o = 16; o > 0; o >>= 1) kmax2 = fmaxf(kmax2, __shfl_xor(kmax2, o));
  float qn2 = 0.f;
#pragma unroll
  for (int i = 0; i < 2; ++i) {
    qn2 += bf_lo(qa[i]) * bf_lo(qa[i]) + bf_hi(qa[i]) * bf_hi(qa[i]);
    qn2 += bf_lo(qb[i]) * bf_lo(qb[i]) + bf_hi(qb[i]) * bf_hi(qb[i]);
    if (h == 0) qn2 += bf_lo(qc[i]) * bf_lo(qc[i]) + bf_hi(qc[i]) * bf_hi(qc[i]);
  }
  qn2 += __shfl_xor(qn2, 32);
  u32x4 b1u; b1u[0] = qa[0]; b1u[1] = qa[1]; b1u[2] = qb[0]; b1u[3] = qb[1];
  const bf16x8 B1 = __builtin_bit_cast(bf16x8, b1u);
  float mshift = sqrtf(qn2 * kmax2);
  const int ka_lane = lane * 16, kb_lane = KV_KB_OFF + lane * 8;
  const int v_lane = KV_V_OFF + h * 352 + (hd < 21 ? hd : 21) * 16;
  f32x16 O;
  for (int pass = 0; pass < 2; ++pass) {
    if (pass == 1) {
      s16x4 B2z; { u32x2 t = qc; if (h) { t[0] = 0; t[1] = 0; } B2z = __builtin_bit_cast(s16x4, t); }
      float mx = -3.0e38f;
      for (int c = 0; c < 8; ++c) {
        attn_stage(lds, kv, c, 0, wid, lane);
        wait_vm<0>(); raw_barrier();
        for (int t = 0; t < 8; ++t) {
          const bf16x8 A1 = *(const bf16x8*)(lds + t * 1024 + ka_lane);
          const s16x4 A2 = *(const s16x4*)(lds + t * 512 + kb_lane);
          f32x16 Sx = {};
          Sx = mfma32(A1, B1, Sx); Sx = mfma32k8(A2, B2z, Sx);
#pragma unroll
          for (int r = 0; r < 16; ++r) mx = fmaxf(mx, Sx[r]);
        }
        raw_barrier();
      }
      mx = fmaxf(mx, __shfl_xor(mx, 32));
      mshift = mx;
      attn_stage(lds, kv, 0, 0, wid, lane);
      attn_stage(lds, kv, 1, 1, wid, lane);
    }
    s16x4 B2;
    {
      const float m0 = bf2f(f2bf(mshift)), m1 = bf2f(f2bf(mshift - m0)), m2 = bf2f(f2bf(mshift - m0 - m1));
      u32x2 t = qc;
      if (h) { t[0] = pk_bf16(-m0, -m1); t[1] = pk_bf16(-m2, 0.f); }
      B2 = __builtin_bit_cast(s16x4, t);
    }
    O = f32x16{};
    f32x16 Sa, Sb;
    bf16x8 KA1; s16x4 KA2;
    auto tile = [&](const f32x16& Sc, f32x16& Sn, const char* vp, const char* k2a, const char* k2b) {
      const bf16x8 V0 = *(const bf16x8*)(vp), V1 = *(const bf16x8*)(vp + 704);
      u32x4 p0, p1;
      p0[0] = pk_bf16(__builtin_amdgcn_exp2f(Sc[0]), __builtin_amdgcn_exp2f(Sc[1]));
      p0[1] = pk_bf16(__builtin_amdgcn_exp2f(Sc[2]), __builtin_amdgcn_exp2f(Sc[3]));
      SB();
      Sn = mfma32(KA1, B1, f32x16{});
      SB();
      p0[2] = pk_bf16(__builtin_amdgcn_exp2f(Sc[4]), __builtin_amdgcn_exp2f(Sc[5]));
      p0[3] = pk_bf16(__builtin_amdgcn_exp2f(Sc[6]), __builtin_amdgcn_exp2f(Sc[7]));
      SB();
      Sn = mfma32k8(KA2, B2, Sn);
      KA1 = *(const bf16x8*)(k2a); KA2 = *(const s16x4*)(k2b);
      SB();
      p1[0] = pk_bf16(__builtin_amdgcn_exp2f(Sc[8]), __builtin_amdgcn_exp2f(Sc[9]));
      p1[1] = pk_bf16(__builtin_amdgcn_exp2f(Sc[10]), __builtin_amdgcn_exp2f(Sc[11]));
      SB();
      O = mfma32(V0, __builtin_bit_cast(bf16x8, p0), O);
      SB();
      p1[2] = pk_bf16(__builtin_amdgcn_exp2f(Sc[12]), __builtin_amdgcn_exp2f(Sc[13]));
      p1[3] = pk_bf16(__builtin_amdgcn_exp2f(Sc[14]), __builtin_amdgcn_exp2f(Sc[15]));
      SB();
      O = mfma32(V1, __builtin_bit_cast(bf16x8, p1), O);
      SB();
    };
    int bc = 0;
    for (int c = 0; c < 8; ++c) {
      wait_vm<0>(); raw_barrier();
      int bn = bc + 1; if (bn == 3) bn = 0;
      int b2n = bn + 1; if (b2n == 3) b2n = 0;
      if (c + 2 < 8) attn_stage(lds, kv, c + 2, b2n, wid, lane);
      const char* l = lds + bc * KV_CHUNK;
      const char* ln = lds + bn * KV_CHUNK;
      if (c == 0) {
        const bf16x8 A1 = *(const bf16x8*)(l + ka_lane);
        const s16x4 A2 = *(const s16x4*)(l + kb_lane);
        KA1 = *(const bf16x8*)(l + 1024 + ka_lane); KA2 = *(const s16x4*)(l + 512 + kb_lane);
        Sa = mfma32(A1, B1, f32x16{}); Sa = mfma32k8(A2, B2, Sa);
      }
#pragma unroll
      for (int t = 0; t < 8; t += 2) {
        const char* ka = t + 2 < 8 ? l + (t + 2) * 1024 : ln + (t - 6) * 1024;
        const char* kb = t + 2 < 8 ? l + (t + 2) * 512 : ln + (t - 6) * 512;
        tile(Sa, Sb, l + v_lane + t * 1408, ka + ka_lane, kb + kb_lane);
        tile(Sb, Sa, l + v_lane + (t + 1) * 1408, ka + 1024 + ka_lane, kb + 512 + kb_lane);
      }
      bc = bn;
    }
    const float lsum = __shfl(O[8], hd + 32);
    const bool bad = !(lsum >= 8.271806e-25f && lsum <= 3.0e38f);
    if (pass == 1) break;
    if (__any(bad) && lane == 0) red[0] = 1.f;
    __syncthreads();
    if (red[0] == 0.f) break;
  }
  {
    const float lsum = __shfl(O[8], hd + 32);
    const float inv = 1.f / lsum;
    u16* orow = (u16*)(ws + WS_O) + (size_t)s * D_ + hd * 20 + 4 * h;
    u32x2 a, b;
    a[0] = pk_bf16(O[0] * inv, O[1] * inv); a[1] = pk_bf16(O[2] * inv, O[3] * inv);
    b[0] = pk_bf16(O[4] * inv, O[5] * inv); b[1] = pk_bf16(O[6] * inv, O[7] * inv);
    typedef __attribute__((address_space(1))) unsigned long long gu64;
    __hip_atomic_store((gu64*)(orow), __builtin_bit_cast(unsigned long long, a), __ATOMIC_RELAXED, __HIP_MEMORY_SCOPE_AGENT);
    __hip_atomic_store((gu64*)(orow + 8), __builtin_bit_cast(unsigned long long, b), __ATOMIC_RELAXED, __HIP_MEMORY_SCOPE_AGENT);
    if (h == 0) {
      u32x2 c; c[0] = pk_bf16(O[8] * inv, O[9] * inv); c[1] = pk_bf16(O[10] * inv, O[11] * inv);
      __hip_atomic_store((gu64*)(orow + 16), __builtin_bit_cast(unsigned long long, c), __ATOMIC_RELAXED, __HIP_MEMORY_SCOPE_AGENT);
    }
    wait_vm<0>();
    __syncthreads();
    if (tid == 0) __hip_atomic_fetch_add((gu32*)(ws + WS_CNT) + (16 + (s >> 6)) * 32, 1u, __ATOMIC_RELAXED, __HIP_MEMORY_SCOPE_AGENT);
  }
}

extern "C" void kernel_launch(void* const* d_in, const int* in_sizes, int n_in, void* d_out, int out_size,
                              void* d_ws, size_t ws_size, hipStream_t stream) {
  const u16* x = (const u16*)d_in[0];
  const u16* wq = (const u16*)d_in[1];
  const u16* wk = (const u16*)d_in[2];
  const u16* wv = (const u16*)d_in[3];
  const u16* wo = (const u16*)d_in[4];
  const u16* w1 = (const u16*)d_in[6];
  const u16* b1 = (const u16*)d_in[7];
  const u16* w2 = (const u16*)d_in[8];
  const u16* b2 = (const u16*)d_in[9];
  const u16* gw = (const u16*)d_in[10];
  char* ws = (char*)d_ws;
  (void)hipFuncSetAttribute((const void*)k_proj, hipFuncAttributeMaxDynamicSharedMemorySize, L1_LDS);
  (void)hipFuncSetAttribute((const void*)k_main, hipFuncAttributeMaxDynamicSharedMemorySize, L2_LDS);
  k_proj<<<L1_NQ + L1_NKV + L1_NLN + L1_NPF, 512, L1_LDS, stream>>>(x, wq, wk, wv, gw, w1, ws);
  k_main<<<512, 512, L2_LDS, stream>>>(w1, b1, w2, b2, wo, ws, (u16*)d_out);
}
```

```cpp
#include <hip/hip_runtime.h>
#include <hip/hip_bf16.h>
#include <stdint.h>

typedef unsigned short u16;
typedef __attribute__((ext_vector_type(8))) short bf16x8;
typedef __attribute__((ext_vector_type(4))) short s16x4;
typedef __attribute__((ext_vector_type(16))) float f32x16;
typedef __attribute__((ext_vector_type(4))) float f32x4;
typedef __attribute__((ext_vector_type(4))) unsigned u32x4;
typedef __attribute__((ext_vector_type(2))) unsigned u32x2;
typedef __attribute__((ext_vector_type(2))) __bf16 bf16x2_t;
#define LDS_AS __attribute__((address_space(3)))
#define DI __device__ __forceinline__
#define SB() __builtin_amdgcn_sched_barrier(0)

constexpr int S_ = 2048, D_ = 640, F_ = 1536;

constexpr size_t WS_CNT = 0;
constexpr size_t WS_Q   = 8192;
constexpr size_t WS_H   = WS_Q + (size_t)S_ * D_ * 2;
constexpr size_t WS_O   = WS_H + (size_t)S_ * D_ * 2;
constexpr size_t WS_U   = WS_O + (size_t)S_ * D_ * 2;
constexpr size_t WS_KV  = WS_U + (size_t)D_ * F_ * 2;
constexpr int    KV_CHUNK = 23552;
constexpr int    KV_KB_OFF = 8192, KV_V_OFF = 12288;
constexpr size_t WS_KN  = WS_KV + 8 * KV_CHUNK;
constexpr size_t WS_G   = WS_KN + 256;

template <bool SC1 = false>
DI void glds16(const void* gbase, unsigned voff, const void* lds_wave_base) {
  const unsigned dst = __builtin_amdgcn_readfirstlane((unsigned)(uintptr_t)lds_wave_base);
  const unsigned lo = __builtin_amdgcn_readfirstlane((unsigned)(uintptr_t)gbase);
  const unsigned hi = __builtin_amdgcn_readfirstlane((unsigned)((uintptr_t)gbase >> 32));
  const unsigned long long gb = ((unsigned long long)hi << 32) | lo;
  unsigned keep;
  if constexpr (SC1)
    asm volatile("s_mov_b32 %0, m0\n\ts_mov_b32 m0, %3\n\ts_nop 0\n\tglobal_load_lds_dwordx4 %1, %2 sc1\n\ts_mov_b32 m0, %0"
                 : "=&s"(keep) : "v"(voff), "s"(gb), "s"(dst) : "memory");
  else
    asm volatile("s_mov_b32 %0, m0\n\ts_mov_b32 m0, %3\n\ts_nop 0\n\tglobal_load_lds_dwordx4 %1, %2\n\ts_mov_b32 m0, %0"
                 : "=&s"(keep) : "v"(voff), "s"(gb), "s"(dst) : "memory");
}
DI unsigned pk_bf16(float a, float b) {
  bf16x2_t p; p[0] = (__bf16)a; p[1] = (__bf16)b; return __builtin_bit_cast(unsigned, p);
}
DI float bf_lo(unsigned u) { return __uint_as_float(u << 16); }
DI float bf_hi(unsigned u) { return __uint_as_float(u & 0xffff0000u); }
DI float bf2f(u16 v) { return __uint_as_float((unsigned)v << 16); }
DI u16 f2bf(float f) { return (u16)(pk_bf16(f, 0.f) & 0xffffu); }
DI f32x16 mfma32(bf16x8 a, bf16x8 b, f32x16 c) { return __builtin_amdgcn_mfma_f32_32x32x16_bf16(a, b, c, 0, 0, 0); }
DI f32x16 mfma32k8(s16x4 a, s16x4 b, f32x16 c) { return __builtin_amdgcn_mfma_f32_32x32x8bf16_1k(a, b, c, 0, 0, 0); }
DI f32x4 mfma16(bf16x8 a, bf16x8 b, f32x4 c) { return __builtin_amdgcn_mfma_f32_16x16x32_bf16(a, b, c, 0, 0, 0); }
DI s16x4 tr_read(const char* p) { return __builtin_amdgcn_ds_read_tr16_b64_v4i16((LDS_AS s16x4*)p); }
DI bf16x8 cat8(s16x4 lo, s16x4 hi) { return __builtin_shufflevector(lo, hi, 0, 1, 2, 3, 4, 5, 6, 7); }
template <int N> DI void wait_vm() { asm volatile("s_waitcnt vmcnt(%0)" :: "i"(N) : "memory"); }
DI void raw_barrier() {
  asm volatile("s_waitcnt lgkmcnt(0)" ::: "memory");
  __builtin_amdgcn_s_barrier();
  asm volatile("" ::: "memory");
}
DI int crow(int r, int h) { return (r & 3) + 8 * (r >> 2) + 4 * h; }

template <int R, int NT, bool SC1 = false> DI void stage_kc(char* lds, const u16* g, int ld, int tid) {
  constexpr int NCH = R * 8, N = (NCH + NT - 1) / NT;
#pragma unroll
  for (int i = 0; i < N; ++i) {
    const int idx = i * NT + tid;
    if ((i + 1) * NT <= NCH || (i * NT + __builtin_amdgcn_readfirstlane(tid & ~63)) < NCH) {
      const int row = idx >> 3, ch = (idx & 7) ^ ((row >> 1) & 7);
      glds16<SC1>(g, (unsigned)(row * ld + ch * 8) * 2u, lds + (i * NT + __builtin_amdgcn_readfirstlane(tid & ~63)) * 16);
    }
  }
}
template <int C> DI int swz_ks(int k) { return C == 64 ? ((((k >> 1) & 1) << 2) | (((k >> 3) & 1) << 1)) : 0; }
template <int KIND> DI unsigned piece_voff(int p, int lane, int ld) {
  const int idx = p * 64 + lane;
  if constexpr (KIND == 0) { const int row = idx >> 3, ch = (idx & 7) ^ ((row >> 1) & 7); return (unsigned)(row * ld + ch * 8) * 2u; }
  else if constexpr (KIND == 1) { const int k = idx >> 3, ch = (idx & 7) ^ swz_ks<64>(k); return (unsigned)(k * ld + ch * 8) * 2u; }
  else { const int k = idx / 10, ch = idx % 10; return (unsigned)(k * ld + ch * 8) * 2u; }
}
template <int AK, int BKD, int NPA, int NPB, bool BSC1>
struct Stager {
  static constexpr int NP = NPA + NPB, BASE = NP / 4, EXTRA = NP % 4, NJ = BASE + (EXTRA ? 1 : 0);
  unsigned voff[NJ]; int p0, n; const u16* base[NJ]; int step[NJ];
  DI void init(const u16* A, int lda, const u16* B, int ldb, int w, int lane) {
    n = BASE + (w < EXTRA ? 1 : 0); p0 = w * BASE + (w < EXTRA ? w : EXTRA);
#pragma unroll
    for (int j = 0; j < NJ; ++j) {
      const int pj = p0 + j;
      const bool isa = pj < NPA;
      voff[j] = isa ? piece_voff<AK>(pj, lane, lda) : piece_voff<BKD>(pj - NPA, lane, ldb);
      base[j] = isa ? A : B;
      step[j] = isa ? (AK == 0 ? 64 : 64 * lda) : (BKD == 0 ? 64 : 64 * ldb);
    }
  }
  DI void stage(char* l, int kt) const {
#pragma unroll
    for (int j = 0; j < NJ; ++j)
      if (j < BASE || j < n) glds16<BSC1>(base[j] + (size_t)kt * step[j], voff[j], l + (p0 + j) * 1024);
  }
  template <int M> DI void wait() const {
    if (n == BASE) wait_vm<BASE * M>(); else wait_vm<(BASE + 1) * M>();
  }
  template <int NBUF, int STG> DI void run(char* lds, int nk) const {
#pragma unroll
    for (int i = 0; i < NBUF - 1; ++i) stage(lds + i * STG, i);
    int bn = NBUF - 1;
    for (int kt = 0; kt < nk; ++kt) {
      if (kt + NBUF - 1 <= nk) wait<NBUF - 2>(); else wait_vm<0>();
      __builtin_amdgcn_s_barrier();
      if (kt + NBUF - 1 < nk) stage(lds + bn * STG, kt + NBUF - 1);
      ++bn; if (bn == NBUF) bn = 0;
    }
  }
};

DI bf16x8 frag32_ks64(const char* lds, int c0, int ks, int lane) {
  const int h = lane >> 5, blk = (lane >> 4) & 1, q = (lane & 15) >> 2, p = lane & 3;
  const int k = 16 * ks + 8 * h + q, ch = ((c0 >> 3) + 2 * blk + (p >> 1)) ^ swz_ks<64>(k);
  const char* a = lds + k * 128 + ch * 16 + 8 * (p & 1);
  return cat8(tr_read(a), tr_read(a + 4 * 128));
}
DI bf16x8 frag32_kc(const char* lds, int row0, int ks, int lane) {
  const int row = row0 + (lane & 31), ch = (2 * ks + (lane >> 5)) ^ ((row >> 1) & 7);
  return *(const bf16x8*)(lds + row * 128 + ch * 16);
}
DI bf16x8 frag16_kc(const char* lds, int row0, int ks, int lane) {
  const int row = row0 + (lane & 15), ch = (4 * ks + (lane >> 4)) ^ ((row >> 1) & 7);
  return *(const bf16x8*)(lds + row * 128 + ch * 16);
}
template <int C> DI bf16x8 frag16_ks(const char* lds, int c0, int ks, int lane) {
  const int g = lane >> 4, q = (lane & 15) >> 2, p = lane & 3;
  const int k = 32 * ks + 8 * g + q, ch = ((c0 >> 3) + (p >> 1)) ^ swz_ks<C>(k);
  const char* a = lds + k * (2 * C) + ch * 16 + 8 * (p & 1);
  return cat8(tr_read(a), tr_read(a + 4 * (2 * C)));
}

constexpr int T80_STG = 64 * 128 + 80 * 128;
template <bool AKC, bool BKC, int NBUF, bool BSC1 = false>
DI void gemm80(f32x4 (&acc)[5], char* lds, const u16* A, int lda, const u16* B, int ldb, int nk, int tid) {
  const int lane = tid & 63, wid = __builtin_amdgcn_readfirstlane(tid >> 6);
  if (wid >= 4) {
    Stager<AKC ? 0 : 1, BKC ? 0 : 2, 8, 10, BSC1> st;
    st.init(A, lda, B, ldb, wid - 4, lane);
    st.template run<NBUF, T80_STG>(lds, nk);
  } else {
    int buf = 0;
    for (int kt = 0; kt < nk; ++kt) {
      raw_barrier();
      bf16x8 af[2], bfr[2][5];
      const char* la = lds + buf * T80_STG;
      const char* lb = la + 8192;
#pragma unroll
      for (int ks = 0; ks < 2; ++ks) {
        if constexpr (AKC) af[ks] = frag16_kc(la, wid * 16, ks, lane); else af[ks] = frag16_ks<64>(la, wid * 16, ks, lane);
#pragma unroll
        for (int j = 0; j < 5; ++j) {
          if constexpr (BKC) bfr[ks][j] = frag16_kc(lb, j * 16, ks, lane); else bfr[ks][j] = frag16_ks<80>(lb, j * 16, ks, lane);
        }
      }
#pragma unroll
      for (int ks = 0; ks < 2; ++ks)
#pragma unroll
        for (int j = 0; j < 5; ++j) {
          acc[j] = mfma16(af[ks], bfr[ks][j], acc[j]);
        }
      ++buf; if (buf == NBUF) buf = 0;
    }
  }
  raw_barrier();
}

constexpr int L1_NQ = 256, L1_NKV = 32, L1_NLN = 64, L1_NPF = 128;
constexpr int L1_LDS = 4 * T80_STG;
constexpr float QSCALE = 0.22360679774997896f * 1.4426950408889634f;

__global__ __launch_bounds__(512) void k_proj(const u16* __restrict__ x, const u16* __restrict__ wq,
                                              const u16* __restrict__ wk, const u16* __restrict__ wv,
                                              const u16* __restrict__ gw, const u16* __restrict__ w1,
                                              const u16* __restrict__ w2, const u16* __restrict__ wo,
                                              char* __restrict__ ws) {
  extern __shared__ __attribute__((aligned(16))) char lds[];
  const int tid = threadIdx.x, lane = tid & 63, wid = tid >> 6;
  const int bid = blockIdx.x;
  if (bid >= L1_NQ + L1_NKV + L1_NLN) {
    const int pb = bid - (L1_NQ + L1_NKV + L1_NLN);
    const int tl = (pb >> 3) * 512 + tid;
    const int t = pb * 512 + tid;
    u32x4 v[24];
    v[16] = *(tl < 1600 ? (const u32x4*)wk + tl : (const u32x4*)wv + (tl < 3200 ? tl - 1600 : 0));
#pragma unroll
    for (int r = 0; r < 3; ++r) { const int c = r * 8192 + tl; v[r] = ((const u32x4*)(x + (size_t)(pb & 7) * 256 * D_))[c < 20480 ? c : 0]; }
#pragma unroll
    for (int r = 0; r < 7; ++r) { const int c = r * 8192 + tl; v[3 + r] = ((const u32x4*)wq)[c < 51200 ? c : 0]; }
#pragma unroll
    for (int r = 0; r < 6; ++r) v[10 + r] = ((const u32x4*)w1)[r * 65536 + t];
#pragma unroll
    for (int r = 0; r < 6; ++r) v[17 + r] = ((const u32x4*)w2)[r * 65536 + t];
    v[23] = ((const u32x4*)wo)[t < 51200 ? t : 0];
#pragma unroll
    for (int r = 0; r < 24; ++r) asm volatile("" :: "v"(v[r]));
    return;
  }
  if (bid < L1_NQ) {
    const int mt = (bid & 7) * 4 + (bid >> 6), nt = (bid >> 3) & 7;
    f32x4 acc[5] = {};
    gemm80<true, false, 4>(acc, lds, x + (size_t)mt * 64 * D_, D_, wq + nt * 80, D_, 10, tid);
    u16* t = (u16*)lds;
    if (wid < 4) {
#pragma unroll
      for (int j = 0; j < 5; ++j)
#pragma unroll
        for (int r = 0; r < 4; ++r)
          t[(wid * 16 + (lane >> 4) * 4 + r) * 80 + j * 16 + (lane & 15)] = f2bf(acc[j][r] * QSCALE);
    }
    __syncthreads();
    u16* q = (u16*)(ws + WS_Q) + (size_t)mt * 64 * D_ + nt * 80;
    for (int i = tid; i < 640; i += 512) {
      const int row = i / 10, ch = i % 10;
      *(u32x4*)(q + (size_t)row * D_ + ch * 8) = *(const u32x4*)(lds + i * 16);
    }
  } else if (bid < L1_NQ + L1_NKV) {
    const int b = bid - L1_NQ;
    const int mt = (b & 7) * 4 + (b >> 3);
    const u16* xa = x + (size_t)mt * 64 * D_;
    const int uw = __builtin_amdgcn_readfirstlane(wid);
    const int wm = (uw >> 1) & 1, isv = uw & 1, h = lane >> 5;
    const int blk = (lane >> 4) & 1, qq = (lane & 15) >> 2, p = lane & 3;
    constexpr int KST = 14336, NBUF = 5;
    f32x16 acc = {};
    if (uw >= 4) {
      auto stage = [&](int kt, int buf) {
        char* l = lds + buf * KST;
        stage_kc<64, 256>(l, xa + kt * 64, D_, tid - 256);
#pragma unroll
        for (int i = 0; i < 2; ++i) {
          const int pc = i * 4 + uw - 4;
          if (pc < 6) {
            const int c = (pc % 3) * 64 + lane;
            glds16((pc < 3 ? wk : wv) + kt * 1280, (unsigned)(c < 160 ? c : 159) * 16u, l + 8192 + pc * 1024);
          }
        }
      };
#pragma unroll
      for (int i = 0; i < NBUF - 1; ++i) stage(i, i);
      int bn = NBUF - 1;
      for (int kt = 0; kt < 10; ++kt) {
        if (kt + NBUF - 1 <= 10) { if (uw < 6) wait_vm<4 * (NBUF - 2)>(); else wait_vm<3 * (NBUF - 2)>(); }
        else wait_vm<0>();
        __builtin_amdgcn_s_barrier();
        if (kt + NBUF - 1 < 10) stage(kt + NBUF - 1, bn);
        ++bn; if (bn == NBUF) bn = 0;
      }
    } else {
      int buf = 0;
      for (int kt = 0; kt < 10; ++kt) {
        raw_barrier();
        const char* la = lds + buf * KST;
        const char* lw = la + 8192 + isv * 3072;
        bf16x8 xf[4], wf[4];
#pragma unroll
        for (int ks = 0; ks < 4; ++ks) {
          xf[ks] = frag32_kc(la, wm * 32, ks, lane);
          const char* a = lw + (16 * ks + 8 * h + qq) * 40 + (16 * blk + 4 * p) * 2;
          wf[ks] = cat8(tr_read(a), tr_read(a + 4 * 40));
        }
        if (isv) {
#pragma unroll
          for (int ks = 0; ks < 4; ++ks) acc = mfma32(xf[ks], wf[ks], acc);
        } else {
#pragma unroll
          for (int ks = 0; ks < 4; ++ks) acc = mfma32(wf[ks], xf[ks], acc);
        }
        ++buf; if (buf == NBUF) buf = 0;
      }
    }
    const int tile = mt * 2 + wm;
    char* img = ws + WS_KV + (size_t)(tile >> 3) * KV_CHUNK;
    const int tl = tile & 7;
    float* red = (float*)lds;
    __syncthreads();
    if (uw >= 4) {
    } else if (!isv) {
      u32x4 a; a[0] = pk_bf16(acc[0], acc[1]); a[1] = pk_bf16(acc[2], acc[3]);
      a[2] = pk_bf16(acc[4], acc[5]); a[3] = pk_bf16(acc[6], acc[7]);
      *(u32x4*)(img + tl * 1024 + lane * 16) = a;
      float n2 = 0.f;
#pragma unroll
      for (int i = 0; i < 4; ++i) { float lo = bf_lo(a[i]), hi = bf_hi(a[i]); n2 += lo * lo + hi * hi; }
      u32x2 bb;
      if (h == 0) {
        bb[0] = pk_bf16(acc[8], acc[9]); bb[1] = pk_bf16(acc[10], acc[11]);
#pragma unroll
        for (int i = 0; i < 2; ++i) { float lo = bf_lo(bb[i]), hi = bf_hi(bb[i]); n2 += lo * lo + hi * hi; }
      } else { bb[0] = 0x3f803f80u; bb[1] = 0x00003f80u; }
      *(u32x2*)(img + KV_KB_OFF + tl * 512 + lane * 8) = bb;
      n2 += __shfl_xor(n2, 32);
#pragma unroll
      for (int o = 16; o > 0; o >>= 1) n2 = fmaxf(n2, __shfl_xor(n2, o));
      if (lane == 0) red[wm] = n2;
    } else {
      const int d = lane & 31;
      if (d <= 21) {
#pragma unroll
        for (int s = 0; s < 2; ++s) {
          u32x4 a;
#pragma unroll
          for (int i = 0; i < 4; ++i) a[i] = d < 20 ? pk_bf16(acc[8 * s + 2 * i], acc[8 * s + 2 * i + 1]) : (d == 20 ? 0x3f803f80u : 0u);
          *(u32x4*)(img + KV_V_OFF + tl * 1408 + (s * 2 + h) * 352 + d * 16) = a;
        }
      }
    }
    __syncthreads();
    if (tid == 0) ((float*)(ws + WS_KN))[mt] = fmaxf(red[0], red[1]);
    if (b == 0 && tid < 48) ((unsigned*)(ws + WS_CNT))[tid * 32] = 0u;
  } else {
    const int b = bid - L1_NQ - L1_NKV;
    const int s0 = (b & 7) * 256 + (b >> 3) * 32 + wid * 4;
    unsigned gwv[5], xv[4][5];
#pragma unroll
    for (int t = 0; t < 4; ++t)
#pragma unroll
      for (int i = 0; i < 5; ++i) xv[t][i] = ((const unsigned*)(x + (size_t)(s0 + t) * D_))[lane + 64 * i];
#pragma unroll
    for (int i = 0; i < 5; ++i) gwv[i] = ((const unsigned*)gw)[lane + 64 * i];
    float sum[4], gs[4], vs[4];
#pragma unroll
    for (int t = 0; t < 4; ++t) {
      sum[t] = 0.f; gs[t] = 0.f;
#pragma unroll
      for (int i = 0; i < 5; ++i) {
        sum[t] += bf_lo(xv[t][i]) + bf_hi(xv[t][i]);
        gs[t] += bf_lo(xv[t][i]) * bf_lo(gwv[i]) + bf_hi(xv[t][i]) * bf_hi(gwv[i]);
      }
    }
#pragma unroll
    for (int o = 32; o > 0; o >>= 1)
#pragma unroll
      for (int t = 0; t < 4; ++t) { sum[t] += __shfl_xor(sum[t], o); gs[t] += __shfl_xor(gs[t], o); }
#pragma unroll
    for (int t = 0; t < 4; ++t) {
      sum[t] *= (1.f / D_); vs[t] = 0.f;
#pragma unroll
      for (int i = 0; i < 5; ++i) {
        const float a = bf_lo(xv[t][i]) - sum[t], c = bf_hi(xv[t][i]) - sum[t];
        vs[t] += a * a + c * c;
      }
    }
#pragma unroll
    for (int o = 32; o > 0; o >>= 1)
#pragma unroll
      for (int t = 0; t < 4; ++t) vs[t] += __shfl_xor(vs[t], o);
#pragma unroll
    for (int t = 0; t < 4; ++t) {
      const float rstd = 1.f / sqrtf(vs[t] * (1.f / D_) + 1e-5f);
      unsigned* hr = (unsigned*)(ws + WS_H + (size_t)(s0 + t) * D_ * 2);
#pragma unroll
      for (int i = 0; i < 5; ++i)
        hr[lane + 64 * i] = pk_bf16((bf_lo(xv[t][i]) - sum[t]) * rstd, (bf_hi(xv[t][i]) - sum[t]) * rstd);
      if (lane == 0) ((float*)(ws + WS_G))[s0 + t] = 1.f / (1.f + __expf(-gs[t]));
    }
  }
}

constexpr int L2_LDS = 4 * T80_STG;

DI float gelu_tanh(float u) {
  const float z = 0.7978845608028654f * (u + 0.044715f * u * u * u);
  const float e = __expf(2.f * z);
  const float t = 1.f - 2.f / (e + 1.f);
  return 0.5f * u * (1.f + t);
}

DI void attn_stage(char* lds, const char* kv, int chunk, int buf, int wid, int lane) {
  const char* g = kv + (size_t)chunk * KV_CHUNK;
  char* l = lds + buf * KV_CHUNK;
#pragma unroll
  for (int r = 0; r < 3; ++r) {
    const int p = r * 8 + wid;
    if (p < 23) glds16(g + p * 1024, (unsigned)lane * 16u, l + p * 1024);
  }
}

typedef __attribute__((address_space(1))) unsigned gu32;

__global__ __launch_bounds__(512, 4) void k_main(const u16* __restrict__ w1, const u16* __restrict__ b1,
                                                 const u16* __restrict__ w2, const u16* __restrict__ b2,
                                                 const u16* __restrict__ wo, char* __restrict__ ws,
                                                 u16* __restrict__ out) {
  extern __shared__ __attribute__((aligned(16))) char lds[];
  const int tid = threadIdx.x, lane = tid & 63, wid = __builtin_amdgcn_readfirstlane(tid >> 6);
  const int bid = blockIdx.x ^ 256;
  if (bid < 256) {
    __builtin_amdgcn_s_setprio(3);
    const int xcd = bid & 7, i = bid >> 3;
    gu32* cnt = (gu32*)(ws + WS_CNT);
    __builtin_amdgcn_s_sleep(25);
    if (i < 30) {
      const int mt = i / 3, nt = xcd * 3 + i % 3;
      const u16* A = (const u16*)(ws + WS_H) + mt * 64;
      const u16* B = w1 + nt * 64;
      const int wm = (wid >> 1) & 1, wn = wid & 1;
      f32x16 acc = {};
      constexpr int NBUF = 4, NK = 32;
      if (wid >= 4) {
        Stager<1, 1, 8, 8, false> st;
        st.init(A, D_, B, F_, wid - 4, lane);
        st.run<NBUF, 16384>(lds, NK);
      } else {
        for (int kt = 0; kt < NK; ++kt) {
          raw_barrier();
          const char* la = lds + (kt & 3) * 16384;
          bf16x8 af[4], bfr[4];
#pragma unroll
          for (int ks = 0; ks < 4; ++ks) { af[ks] = frag32_ks64(la, wm * 32, ks, lane); bfr[ks] = frag32_ks64(la + 8192, wn * 32, ks, lane); }
#pragma unroll
          for (int ks = 0; ks < 4; ++ks) {
            acc = mfma32(af[ks], bfr[ks], acc);
          }
        }
      }
      raw_barrier();
      u16* t = (u16*)(lds + 16384);
      if (wid < 4) {
        const int h = lane >> 5, fl = wn * 32 + (lane & 31);
        const float bb = bf2f(b1[nt * 64 + fl]);
#pragma unroll
        for (int r = 0; r < 16; ++r) t[(wm * 32 + crow(r, h)) * 64 + fl] = f2bf(gelu_tanh(acc[r] + bb));
      }
      __syncthreads();
      {
        const int row = tid >> 3, ch = tid & 7;
        const u32x4 v = *(const u32x4*)(lds + 16384 + tid * 16);
        u16* dst = (u16*)(ws + WS_U) + (size_t)(mt * 64 + row) * F_ + nt * 64 + ch * 8;
        asm volatile("global_store_dwordx4 %0, %1, off sc1\n\ts_nop 1" :: "v"(dst), "v"(v) : "memory");
      }
      wait_vm<0>();
      __syncthreads();
      if (tid == 0) __hip_atomic_fetch_add(cnt + mt * 32, 1u, __ATOMIC_RELAXED, __HIP_MEMORY_SCOPE_AGENT);
    }
    if (wid == 0) {
      for (int spin = 0; spin < (1 << 22); ++spin) {
        const unsigned c = __hip_atomic_load(cnt + (lane < 10 ? lane : 0) * 32, __ATOMIC_RELAXED, __HIP_MEMORY_SCOPE_AGENT);
        if (__all(c >= 24u)) break;
        __builtin_amdgcn_s_sleep(2);
      }
    }
    __syncthreads();
    {
      const int mt = xcd * 4 + (i >> 3), nt = i & 7;
      f32x4 acc[5] = {};
      gemm80<false, true, 4, true>(acc, lds, w2 + mt * 64, S_, (const u16*)(ws + WS_U) + (size_t)nt * 80 * F_, F_, 24, tid);
      if (wid == 0) {
        for (int spin = 0; spin < (1 << 22); ++spin) {
          const unsigned c = __hip_atomic_load(cnt + (16 + mt) * 32, __ATOMIC_RELAXED, __HIP_MEMORY_SCOPE_AGENT);
          if (c >= 8u) break;
          __builtin_amdgcn_s_sleep(4);
        }
      }
      float gv[4], bv[4];
      if (wid < 4) {
#pragma unroll
        for (int r = 0; r < 4; ++r) {
          const int row = mt * 64 + wid * 16 + (lane >> 4) * 4 + r;
          gv[r] = ((const float*)(ws + WS_G))[row]; bv[r] = bf2f(b2[row]);
        }
      }
      __syncthreads();
      f32x4 acca[5] = {};
      gemm80<true, false, 4, true>(acca, lds, (const u16*)(ws + WS_O) + (size_t)mt * 64 * D_, D_, wo + nt * 80, D_, 10, tid);
      u16* t = (u16*)lds;
      if (wid < 4) {
#pragma unroll
        for (int r = 0; r < 4; ++r) {
          const int row = wid * 16 + (lane >> 4) * 4 + r;
#pragma unroll
          for (int j = 0; j < 5; ++j)
            t[row * 80 + j * 16 + (lane & 15)] = f2bf(gv[r] * acca[j][r] + (1.f - gv[r]) * (acc[j][r] + bv[r]));
        }
      }
      __syncthreads();
      u16* o = out + (size_t)mt * 64 * D_ + nt * 80;
      for (int c = tid; c < 640; c += 512) {
        const int row = c / 10, ch = c % 10;
        *(u32x4*)(o + (size_t)row * D_ + ch * 8) = *(const u32x4*)(lds + c * 16);
      }
    }
    return;
  }
  const int ab = bid - 256;
  const int s = ((ab & 7) * 32 + (ab >> 3)) * 8 + wid;
  const int h = lane >> 5, hd = lane & 31;
  const char* kv = ws + WS_KV;
  float* red = (float*)(lds + 3 * KV_CHUNK);
  if (tid == 0) red[0] = 0.f;
  const u16* qr = (const u16*)(ws + WS_Q) + (size_t)s * D_ + hd * 20;
  const u32x2 qa = *(const u32x2*)(qr + 4 * h), qb = *(const u32x2*)(qr + 8 + 4 * h);
  const u32x2 qc = *(const u32x2*)(qr + 16);
  float kmax2 = ((const float*)(ws + WS_KN))[hd];
  attn_stage(lds, kv, 0, 0, wid, lane);
  attn_stage(lds, kv, 1, 1, wid, lane);
#pragma unroll
  for (int o = 16; o > 0; o >>= 1) kmax2 = fmaxf(kmax2, __shfl_xor(kmax2, o));
  float qn2 = 0.f;
#pragma unroll
  for (int i = 0; i < 2; ++i) {
    qn2 += bf_lo(qa[i]) * bf_lo(qa[i]) + bf_hi(qa[i]) * bf_hi(qa[i]);
    qn2 += bf_lo(qb[i]) * bf_lo(qb[i]) + bf_hi(qb[i]) * bf_hi(qb[i]);
    if (h == 0) qn2 += bf_lo(qc[i]) * bf_lo(qc[i]) + bf_hi(qc[i]) * bf_hi(qc[i]);
  }
  qn2 += __shfl_xor(qn2, 32);
  u32x4 b1u; b1u[0] = qa[0]; b1u[1] = qa[1]; b1u[2] = qb[0]; b1u[3] = qb[1];
  const bf16x8 B1 = __builtin_bit_cast(bf16x8, b1u);
  float mshift = sqrtf(qn2 * kmax2);
  const int ka_lane = lane * 16, kb_lane = KV_KB_OFF + lane * 8;
  const int v_lane = KV_V_OFF + h * 352 + (hd < 21 ? hd : 21) * 16;
  f32x16 O;
  for (int pass = 0; pass < 2; ++pass) {
    if (pass == 1) {
      s16x4 B2z; { u32x2 t = qc; if (h) { t[0] = 0; t[1] = 0; } B2z = __builtin_bit_cast(s16x4, t); }
      float mx = -3.0e38f;
      for (int c = 0; c < 8; ++c) {
        attn_stage(lds, kv, c, 0, wid, lane);
        wait_vm<0>(); raw_barrier();
        for (int t = 0; t < 8; ++t) {
          const bf16x8 A1 = *(const bf16x8*)(lds + t * 1024 + ka_lane);
          const s16x4 A2 = *(const s16x4*)(lds + t * 512 + kb_lane);
          f32x16 Sx = {};
          Sx = mfma32(A1, B1, Sx); Sx = mfma32k8(A2, B2z, Sx);
#pragma unroll
          for (int r = 0; r < 16; ++r) mx = fmaxf(mx, Sx[r]);
        }
        raw_barrier();
      }
      mx = fmaxf(mx, __shfl_xor(mx, 32));
      mshift = mx;
      attn_stage(lds, kv, 0, 0, wid, lane);
      attn_stage(lds, kv, 1, 1, wid, lane);
    }
    s16x4 B2;
    {
      const float m0 = bf2f(f2bf(mshift)), m1 = bf2f(f2bf(mshift - m0)), m2 = bf2f(f2bf(mshift - m0 - m1));
      u32x2 t = qc;
      if (h) { t[0] = pk_bf16(-m0, -m1); t[1] = pk_bf16(-m2, 0.f); }
      B2 = __builtin_bit_cast(s16x4, t);
    }
    O = f32x16{};
    f32x16 Sa, Sb;
    bf16x8 KA1; s16x4 KA2;
    auto tile = [&](const f32x16& Sc, f32x16& Sn, const char* vp, const char* k2a, const char* k2b) {
      const bf16x8 V0 = *(const bf16x8*)(vp), V1 = *(const bf16x8*)(vp + 704);
      u32x4 p0, p1;
      p0[0] = pk_bf16(__builtin_amdgcn_exp2f(Sc[0]), __builtin_amdgcn_exp2f(Sc[1]));
      p0[1] = pk_bf16(__builtin_amdgcn_exp2f(Sc[2]), __builtin_amdgcn_exp2f(Sc[3]));
      SB();
      Sn = mfma32(KA1, B1, f32x16{});
      SB();
      p0[2] = pk_bf16(__builtin_amdgcn_exp2f(Sc[4]), __builtin_amdgcn_exp2f(Sc[5]));
      p0[3] = pk_bf16(__builtin_amdgcn_exp2f(Sc[6]), __builtin_amdgcn_exp2f(Sc[7]));
      SB();
      Sn = mfma32k8(KA2, B2, Sn);
      KA1 = *(const bf16x8*)(k2a); KA2 = *(const s16x4*)(k2b);
      SB();
      p1[0] = pk_bf16(__builtin_amdgcn_exp2f(Sc[8]), __builtin_amdgcn_exp2f(Sc[9]));
      p1[1] = pk_bf16(__builtin_amdgcn_exp2f(Sc[10]), __builtin_amdgcn_exp2f(Sc[11]));
      SB();
      O = mfma32(V0, __builtin_bit_cast(bf16x8, p0), O);
      SB();
      p1[2] = pk_bf16(__builtin_amdgcn_exp2f(Sc[12]), __builtin_amdgcn_exp2f(Sc[13]));
      p1[3] = pk_bf16(__builtin_amdgcn_exp2f(Sc[14]), __builtin_amdgcn_exp2f(Sc[15]));
      SB();
      O = mfma32(V1, __builtin_bit_cast(bf16x8, p1), O);
      SB();
    };
    int bc = 0;
    for (int c = 0; c < 8; ++c) {
      wait_vm<0>(); raw_barrier();
      int bn = bc + 1; if (bn == 3) bn = 0;
      int b2n = bn + 1; if (b2n == 3) b2n = 0;
      if (c + 2 < 8) attn_stage(lds, kv, c + 2, b2n, wid, lane);
      const char* l = lds + bc * KV_CHUNK;
      const char* ln = lds + bn * KV_CHUNK;
      if (c == 0) {
        const bf16x8 A1 = *(const bf16x8*)(l + ka_lane);
        const s16x4 A2 = *(const s16x4*)(l + kb_lane);
        KA1 = *(const bf16x8*)(l + 1024 + ka_lane); KA2 = *(const s16x4*)(l + 512 + kb_lane);
        Sa = mfma32(A1, B1, f32x16{}); Sa = mfma32k8(A2, B2, Sa);
      }
#pragma unroll
      for (int t = 0; t < 8; t += 2) {
        const char* ka = t + 2 < 8 ? l + (t + 2) * 1024 : ln + (t - 6) * 1024;
        const char* kb = t + 2 < 8 ? l + (t + 2) * 512 : ln + (t - 6) * 512;
        tile(Sa, Sb, l + v_lane + t * 1408, ka + ka_lane, kb + kb_lane);
        tile(Sb, Sa, l + v_lane + (t + 1) * 1408, ka + 1024 + ka_lane, kb + 512 + kb_lane);
      }
      bc = bn;
    }
    const float lsum = __shfl(O[8], hd + 32);
    const bool bad = !(lsum >= 8.271806e-25f && lsum <= 3.0e38f);
    if (pass == 1) break;
    if (__any(bad) && lane == 0) red[0] = 1.f;
    __syncthreads();
    if (red[0] == 0.f) break;
  }
  {
    const float lsum = __shfl(O[8], hd + 32);
    const float inv = 1.f / lsum;
    u16* orow = (u16*)(ws + WS_O) + (size_t)s * D_ + hd * 20 + 4 * h;
    u32x2 a, b;
    a[0] = pk_bf16(O[0] * inv, O[1] * inv); a[1] = pk_bf16(O[2] * inv, O[3] * inv);
    b[0] = pk_bf16(O[4] * inv, O[5] * inv); b[1] = pk_bf16(O[6] * inv, O[7] * inv);
    typedef __attribute__((address_space(1))) unsigned long long gu64;
    __hip_atomic_store((gu64*)(orow), __builtin_bit_cast(unsigned long long, a), __ATOMIC_RELAXED, __HIP_MEMORY_SCOPE_AGENT);
    __hip_atomic_store((gu64*)(orow + 8), __builtin_bit_cast(unsigned long long, b), __ATOMIC_RELAXED, __HIP_MEMORY_SCOPE_AGENT);
    if (h == 0) {
      u32x2 c; c[0] = pk_bf16(O[8] * inv, O[9] * inv); c[1] = pk_bf16(O[10] * inv, O[11] * inv);
      __hip_atomic_store((gu64*)(orow + 16), __builtin_bit_cast(unsigned long long, c), __ATOMIC_RELAXED, __HIP_MEMORY_SCOPE_AGENT);
    }
    wait_vm<0>();
    __syncthreads();
    if (tid == 0) __hip_atomic_fetch_add((gu32*)(ws + WS_CNT) + (16 + (s >> 6)) * 32, 1u, __ATOMIC_RELAXED, __HIP_MEMORY_SCOPE_AGENT);
  }
}

extern "C" void kernel_launch(void* const* d_in, const int* in_sizes, int n_in, void* d_out, int out_size,
                              void* d_ws, size_t ws_size, hipStream_t stream) {
  const u16* x = (const u16*)d_in[0];
  const u16* wq = (const u16*)d_in[1];
  const u16* wk = (const u16*)d_in[2];
  const u16* wv = (const u16*)d_in[3];
  const u16* wo = (const u16*)d_in[4];
  const u16* w1 = (const u16*)d_in[6];
  const u16* b1 = (const u16*)d_in[7];
  const u16* w2 = (const u16*)d_in[8];
  const u16* b2 = (const u16*)d_in[9];
  const u16* gw = (const u16*)d_in[10];
  char* ws = (char*)d_ws;
  (void)hipFuncSetAttribute((const void*)k_proj, hipFuncAttributeMaxDynamicSharedMemorySize, L1_LDS);
  (void)hipFuncSetAttribute((const void*)k_main, hipFuncAttributeMaxDynamicSharedMemorySize, L2_LDS);
  k_proj<<<L1_NQ + L1_NKV + L1_NLN + L1_NPF, 512, L1_LDS, stream>>>(x, wq, wk, wv, gw, w1, w2, wo, ws);
  k_main<<<512, 512, L2_LDS, stream>>>(w1, b1, w2, b2, wo, ws, (u16*)d_out);
}
```

```cpp
#include <hip/hip_runtime.h>
#include <hip/hip_bf16.h>
#include <stdint.h>

typedef unsigned short u16;
typedef __attribute__((ext_vector_type(8))) short bf16x8;
typedef __attribute__((ext_vector_type(4))) short s16x4;
typedef __attribute__((ext_vector_type(16))) float f32x16;
typedef __attribute__((ext_vector_type(4))) float f32x4;
typedef __attribute__((ext_vector_type(4))) unsigned u32x4;
typedef __attribute__((ext_vector_type(2))) unsigned u32x2;
typedef __attribute__((ext_vector_type(2))) __bf16 bf16x2_t;
#define LDS_AS __attribute__((address_space(3)))
#define DI __device__ __forceinline__
#define SB() __builtin_amdgcn_sched_barrier(0)

constexpr int S_ = 2048, D_ = 640, F_ = 1536;

constexpr size_t WS_CNT = 0;
constexpr size_t WS_Q   = 8192;
constexpr size_t WS_H   = WS_Q + (size_t)S_ * D_ * 2;
constexpr size_t WS_O   = WS_H + (size_t)S_ * D_ * 2;
constexpr size_t WS_U   = WS_O + (size_t)S_ * D_ * 2;
constexpr size_t WS_KV  = WS_U + (size_t)D_ * F_ * 2;
constexpr int    KV_CHUNK = 23552;
constexpr int    KV_KB_OFF = 8192, KV_V_OFF = 12288;
constexpr size_t WS_KN  = WS_KV + 8 * KV_CHUNK;
constexpr size_t WS_G   = WS_KN + 256;

template <bool SC1 = false>
DI void glds16(const void* gbase, unsigned voff, const void* lds_wave_base) {
  const unsigned dst = __builtin_amdgcn_readfirstlane((unsigned)(uintptr_t)lds_wave_base);
  const unsigned lo = __builtin_amdgcn_readfirstlane((unsigned)(uintptr_t)gbase);
  const unsigned hi = __builtin_amdgcn_readfirstlane((unsigned)((uintptr_t)gbase >> 32));
  const unsigned long long gb = ((unsigned long long)hi << 32) | lo;
  unsigned keep;
  if constexpr (SC1)
    asm volatile("s_mov_b32 %0, m0\n\ts_mov_b32 m0, %3\n\ts_nop 0\n\tglobal_load_lds_dwordx4 %1, %2 sc1\n\ts_mov_b32 m0, %0"
                 : "=&s"(keep) : "v"(voff), "s"(gb), "s"(dst) : "memory");
  else
    asm volatile("s_mov_b32 %0, m0\n\ts_mov_b32 m0, %3\n\ts_nop 0\n\tglobal_load_lds_dwordx4 %1, %2\n\ts_mov_b32 m0, %0"
                 : "=&s"(keep) : "v"(voff), "s"(gb), "s"(dst) : "memory");
}
DI unsigned pk_bf16(float a, float b) {
  bf16x2_t p; p[0] = (__bf16)a; p[1] = (__bf16)b; return __builtin_bit_cast(unsigned, p);
}
DI float bf_lo(unsigned u) { return __uint_as_float(u << 16); }
DI float bf_hi(unsigned u) { return __uint_as_float(u & 0xffff0000u); }
DI float bf2f(u16 v) { return __uint_as_float((unsigned)v << 16); }
DI u16 f2bf(float f) { return (u16)(pk_bf16(f, 0.f) & 0xffffu); }
DI f32x16 mfma32(bf16x8 a, bf16x8 b, f32x16 c) { return __builtin_amdgcn_mfma_f32_32x32x16_bf16(a, b, c, 0, 0, 0); }
DI f32x16 mfma32k8(s16x4 a, s16x4 b, f32x16 c) { return __builtin_amdgcn_mfma_f32_32x32x8bf16_1k(a, b, c, 0, 0, 0); }
DI f32x4 mfma16(bf16x8 a, bf16x8 b, f32x4 c) { return __builtin_amdgcn_mfma_f32_16x16x32_bf16(a, b, c, 0, 0, 0); }
DI s16x4 tr_read(const char* p) { return __builtin_amdgcn_ds_read_tr16_b64_v4i16((LDS_AS s16x4*)p); }
DI bf16x8 cat8(s16x4 lo, s16x4 hi) { return __builtin_shufflevector(lo, hi, 0, 1, 2, 3, 4, 5, 6, 7); }
template <int N> DI void wait_vm() { asm volatile("s_waitcnt vmcnt(%0)" :: "i"(N) : "memory"); }
DI void raw_barrier() {
  asm volatile("s_waitcnt lgkmcnt(0)" ::: "memory");
  __builtin_amdgcn_s_barrier();
  asm volatile("" ::: "memory");
}
DI int crow(int r, int h) { return (r & 3) + 8 * (r >> 2) + 4 * h; }

template <int R, int NT, bool SC1 = false> DI void stage_kc(char* lds, const u16* g, int ld, int tid) {
  constexpr int NCH = R * 8, N = (NCH + NT - 1) / NT;
#pragma unroll
  for (int i = 0; i < N; ++i) {
    const int idx = i * NT + tid;
    if ((i + 1) * NT <= NCH || (i * NT + __builtin_amdgcn_readfirstlane(tid & ~63)) < NCH) {
      const int row = idx >> 3, ch = (idx & 7) ^ ((row >> 1) & 7);
      glds16<SC1>(g, (unsigned)(row * ld + ch * 8) * 2u, lds + (i * NT + __builtin_amdgcn_readfirstlane(tid & ~63)) * 16);
    }
  }
}
template <int C> DI int swz_ks(int k) { return C == 64 ? ((((k >> 1) & 1) << 2) | (((k >> 3) & 1) << 1)) : 0; }
template <int KIND> DI unsigned piece_voff(int p, int lane, int ld) {
  const int idx = p * 64 + lane;
  if constexpr (KIND == 0) { const int row = idx >> 3, ch = (idx & 7) ^ ((row >> 1) & 7); return (unsigned)(row * ld + ch * 8) * 2u; }
  else if constexpr (KIND == 1) { const int k = idx >> 3, ch = (idx & 7) ^ swz_ks<64>(k); return (unsigned)(k * ld + ch * 8) * 2u; }
  else { const int k = idx / 10, ch = idx % 10; return (unsigned)(k * ld + ch * 8) * 2u; }
}
template <int AK, int BKD, int NPA, int NPB, bool BSC1>
struct Stager {
  static constexpr int NP = NPA + NPB, BASE = NP / 4, EXTRA = NP % 4, NJ = BASE + (EXTRA ? 1 : 0);
  unsigned voff[NJ]; int p0, n; const u16* base[NJ]; int step[NJ];
  DI void init(const u16* A, int lda, const u16* B, int ldb, int w, int lane) {
    n = BASE + (w < EXTRA ? 1 : 0); p0 = w * BASE + (w < EXTRA ? w : EXTRA);
#pragma unroll
    for (int j = 0; j < NJ; ++j) {
      const int pj = p0 + j;
      const bool isa = pj < NPA;
      voff[j] = isa ? piece_voff<AK>(pj, lane, lda) : piece_voff<BKD>(pj - NPA, lane, ldb);
      base[j] = isa ? A : B;
      step[j] = isa ? (AK == 0 ? 64 : 64 * lda) : (BKD == 0 ? 64 : 64 * ldb);
    }
  }
  DI void stage(char* l, int kt) const {
#pragma unroll
    for (int j = 0; j < NJ; ++j)
      if (j < BASE || j < n) glds16<BSC1>(base[j] + (size_t)kt * step[j], voff[j], l + (p0 + j) * 1024);
  }
  template <int M> DI void wait() const {
    if (n == BASE) wait_vm<BASE * M>(); else wait_vm<(BASE + 1) * M>();
  }
  template <int NBUF, int STG> DI void run(char* lds, int nk) const {
#pragma unroll
    for (int i = 0; i < NBUF - 1; ++i) stage(lds + i * STG, i);
    int bn = NBUF - 1;
    for (int kt = 0; kt < nk; ++kt) {
      if (kt + NBUF - 1 <= nk) wait<NBUF - 2>(); else wait_vm<0>();
      __builtin_amdgcn_s_barrier();
      if (kt + NBUF - 1 < nk) stage(lds + bn * STG, kt + NBUF - 1);
      ++bn; if (bn == NBUF) bn = 0;
    }
  }
};

DI bf16x8 frag32_ks64(const char* lds, int c0, int ks, int lane) {
  const int h = lane >> 5, blk = (lane >> 4) & 1, q = (lane & 15) >> 2, p = lane & 3;
  const int k = 16 * ks + 8 * h + q, ch = ((c0 >> 3) + 2 * blk + (p >> 1)) ^ swz_ks<64>(k);
  const char* a = lds + k * 128 + ch * 16 + 8 * (p & 1);
  return cat8(tr_read(a), tr_read(a + 4 * 128));
}
DI bf16x8 frag32_kc(const char* lds, int row0, int ks, int lane) {
  const int row = row0 + (lane & 31), ch = (2 * ks + (lane >> 5)) ^ ((row >> 1) & 7);
  return *(const bf16x8*)(lds + row * 128 + ch * 16);
}
DI bf16x8 frag16_kc(const char* lds, int row0, int ks, int lane) {
  const int row = row0 + (lane & 15), ch = (4 * ks + (lane >> 4)) ^ ((row >> 1) & 7);
  return *(const bf16x8*)(lds + row * 128 + ch * 16);
}
template <int C> DI bf16x8 frag16_ks(const char* lds, int c0, int ks, int lane) {
  const int g = lane >> 4, q = (lane & 15) >> 2, p = lane & 3;
  const int k = 32 * ks + 8 * g + q, ch = ((c0 >> 3) + (p >> 1)) ^ swz_ks<C>(k);
  const char* a = lds + k * (2 * C) + ch * 16 + 8 * (p & 1);
  return cat8(tr_read(a), tr_read(a + 4 * (2 * C)));
}

constexpr int T80_STG = 64 * 128 + 80 * 128;
template <bool AKC, bool BKC, int NBUF, bool BSC1 = false>
DI void gemm80(f32x4 (&acc)[5], char* lds, const u16* A, int lda, const u16* B, int ldb, int nk, int tid) {
  const int lane = tid & 63, wid = __builtin_amdgcn_readfirstlane(tid >> 6);
  if (wid >= 4) {
    Stager<AKC ? 0 : 1, BKC ? 0 : 2, 8, 10, BSC1> st;
    st.init(A, lda, B, ldb, wid - 4, lane);
    st.template run<NBUF, T80_STG>(lds, nk);
  } else {
    int buf = 0;
    for (int kt = 0; kt < nk; ++kt) {
      raw_barrier();
      bf16x8 af[2], bfr[2][5];
      const char* la = lds + buf * T80_STG;
      const char* lb = la + 8192;
#pragma unroll
      for (int ks = 0; ks < 2; ++ks) {
        if constexpr (AKC) af[ks] = frag16_kc(la, wid * 16, ks, lane); else af[ks] = frag16_ks<64>(la, wid * 16, ks, lane);
#pragma unroll
        for (int j = 0; j < 5; ++j) {
          if constexpr (BKC) bfr[ks][j] = frag16_kc(lb, j * 16, ks, lane); else bfr[ks][j] = frag16_ks<80>(lb, j * 16, ks, lane);
        }
      }
#pragma unroll
      for (int ks = 0; ks < 2; ++ks)
#pragma unroll
        for (int j = 0; j < 5; ++j) {
          acc[j] = mfma16(af[ks], bfr[ks][j], acc[j]);
        }
      ++buf; if (buf == NBUF) buf = 0;
    }
  }
  raw_barrier();
}

constexpr int L1_NQ = 256, L1_NKV = 32, L1_NLN = 64, L1_NPF = 128;
constexpr int L1_LDS = 4 * T80_STG;
constexpr float QSCALE = 0.22360679774997896f * 1.4426950408889634f;

__global__ __launch_bounds__(512) void k_proj(const u16* __restrict__ x, const u16* __restrict__ wq,
                                              const u16* __restrict__ wk, const u16* __restrict__ wv,
                                              const u16* __restrict__ gw, const u16* __restrict__ w1,
                                              char* __restrict__ ws) {
  extern __shared__ __attribute__((aligned(16))) char lds[];
  const int tid = threadIdx.x, lane = tid & 63, wid = tid >> 6;
  const int bid = blockIdx.x;
  if (bid >= L1_NQ + L1_NKV + L1_NLN) {
    const int pb = bid - (L1_NQ + L1_NKV + L1_NLN);
    const int tl = (pb >> 3) * 512 + tid;
    const int t = pb * 512 + tid;
    u32x4 v[17];
    v[16] = *(tl < 1600 ? (const u32x4*)wk + tl : (const u32x4*)wv + (tl < 3200 ? tl - 1600 : 0));
#pragma unroll
    for (int r = 0; r < 3; ++r) { const int c = r * 8192 + tl; v[r] = ((const u32x4*)(x + (size_t)(pb & 7) * 256 * D_))[c < 20480 ? c : 0]; }
#pragma unroll
    for (int r = 0; r < 7; ++r) { const int c = r * 8192 + tl; v[3 + r] = ((const u32x4*)wq)[c < 51200 ? c : 0]; }
#pragma unroll
    for (int r = 0; r < 6; ++r) v[10 + r] = ((const u32x4*)w1)[r * 65536 + t];
#pragma unroll
    for (int r = 0; r < 17; ++r) asm volatile("" :: "v"(v[r]));
    return;
  }
  if (bid < L1_NQ) {
    const int mt = (bid & 7) * 4 + (bid >> 6), nt = (bid >> 3) & 7;
    f32x4 acc[5] = {};
    gemm80<true, false, 4>(acc, lds, x + (size_t)mt * 64 * D_, D_, wq + nt * 80, D_, 10, tid);
    u16* t = (u16*)lds;
    if (wid < 4) {
#pragma unroll
      for (int j = 0; j < 5; ++j)
#pragma unroll
        for (int r = 0; r < 4; ++r)
          t[(wid * 16 + (lane >> 4) * 4 + r) * 80 + j * 16 + (lane & 15)] = f2bf(acc[j][r] * QSCALE);
    }
    __syncthreads();
    u16* q = (u16*)(ws + WS_Q) + (size_t)mt * 64 * D_ + nt * 80;
    for (int i = tid; i < 640; i += 512) {
      const int row = i / 10, ch = i % 10;
      *(u32x4*)(q + (size_t)row * D_ + ch * 8) = *(const u32x4*)(lds + i * 16);
    }
  } else if (bid < L1_NQ + L1_NKV) {
    const int b = bid - L1_NQ;
    const int mt = (b & 7) * 4 + (b >> 3);
    const u16* xa = x + (size_t)mt * 64 * D_;
    const int uw = __builtin_amdgcn_readfirstlane(wid);
    const int wm = (uw >> 1) & 1, isv = uw & 1, h = lane >> 5;
    const int blk = (lane >> 4) & 1, qq = (lane & 15) >> 2, p = lane & 3;
    constexpr int KST = 14336, NBUF = 5;
    f32x16 acc = {};
    if (uw >= 4) {
      auto stage = [&](int kt, int buf) {
        char* l = lds + buf * KST;
        stage_kc<64, 256>(l, xa + kt * 64, D_, tid - 256);
#pragma unroll
        for (int i = 0; i < 2; ++i) {
          const int pc = i * 4 + uw - 4;
          if (pc < 6) {
            const int c = (pc % 3) * 64 + lane;
            glds16((pc < 3 ? wk : wv) + kt * 1280, (unsigned)(c < 160 ? c : 159) * 16u, l + 8192 + pc * 1024);
          }
        }
      };
#pragma unroll
      for (int i = 0; i < NBUF - 1; ++i) stage(i, i);
      int bn = NBUF - 1;
      for (int kt = 0; kt < 10; ++kt) {
        if (kt + NBUF - 1 <= 10) { if (uw < 6) wait_vm<4 * (NBUF - 2)>(); else wait_vm<3 * (NBUF - 2)>(); }
        else wait_vm<0>();
        __builtin_amdgcn_s_barrier();
        if (kt + NBUF - 1 < 10) stage(kt + NBUF - 1, bn);
        ++bn; if (bn == NBUF) bn = 0;
      }
    } else {
      int buf = 0;
      for (int kt = 0; kt < 10; ++kt) {
        raw_barrier();
        const char* la = lds + buf * KST;
        const char* lw = la + 8192 + isv * 3072;
        bf16x8 xf[4], wf[4];
#pragma unroll
        for (int ks = 0; ks < 4; ++ks) {
          xf[ks] = frag32_kc(la, wm * 32, ks, lane);
          const char* a = lw + (16 * ks + 8 * h + qq) * 40 + (16 * blk + 4 * p) * 2;
          wf[ks] = cat8(tr_read(a), tr_read(a + 4 * 40));
        }
        if (isv) {
#pragma unroll
          for (int ks = 0; ks < 4; ++ks) acc = mfma32(xf[ks], wf[ks], acc);
        } else {
#pragma unroll
          for (int ks = 0; ks < 4; ++ks) acc = mfma32(wf[ks], xf[ks], acc);
        }
        ++buf; if (buf == NBUF) buf = 0;
      }
    }
    const int tile = mt * 2 + wm;
    char* img = ws + WS_KV + (size_t)(tile >> 3) * KV_CHUNK;
    const int tl = tile & 7;
    float* red = (float*)lds;
    __syncthreads();
    if (uw >= 4) {
    } else if (!isv) {
      u32x4 a; a[0] = pk_bf16(acc[0], acc[1]); a[1] = pk_bf16(acc[2], acc[3]);
      a[2] = pk_bf16(acc[4], acc[5]); a[3] = pk_bf16(acc[6], acc[7]);
      *(u32x4*)(img + tl * 1024 + lane * 16) = a;
      float n2 = 0.f;
#pragma unroll
      for (int i = 0; i < 4; ++i) { float lo = bf_lo(a[i]), hi = bf_hi(a[i]); n2 += lo * lo + hi * hi; }
      u32x2 bb;
      if (h == 0) {
        bb[0] = pk_bf16(acc[8], acc[9]); bb[1] = pk_bf16(acc[10], acc[11]);
#pragma unroll
        for (int i = 0; i < 2; ++i) { float lo = bf_lo(bb[i]), hi = bf_hi(bb[i]); n2 += lo * lo + hi * hi; }
      } else { bb[0] = 0x3f803f80u; bb[1] = 0x00003f80u; }
      *(u32x2*)(img + KV_KB_OFF + tl * 512 + lane * 8) = bb;
      n2 += __shfl_xor(n2, 32);
#pragma unroll
      for (int o = 16; o > 0; o >>= 1) n2 = fmaxf(n2, __shfl_xor(n2, o));
      if (lane == 0) red[wm] = n2;
    } else {
      const int d = lane & 31;
      if (d <= 21) {
#pragma unroll
        for (int s = 0; s < 2; ++s) {
          u32x4 a;
#pragma unroll
          for (int i = 0; i < 4; ++i) a[i] = d < 20 ? pk_bf16(acc[8 * s + 2 * i], acc[8 * s + 2 * i + 1]) : (d == 20 ? 0x3f803f80u : 0u);
          *(u32x4*)(img + KV_V_OFF + tl * 1408 + (s * 2 + h) * 352 + d * 16) = a;
        }
      }
    }
    __syncthreads();
    if (tid == 0) ((float*)(ws + WS_KN))[mt] = fmaxf(red[0], red[1]);
    if (b == 0 && tid < 48) ((unsigned*)(ws + WS_CNT))[tid * 32] = 0u;
  } else {
    const int b = bid - L1_NQ - L1_NKV;
    const int s0 = (b & 7) * 256 + (b >> 3) * 32 + wid * 4;
    unsigned gwv[5], xv[4][5];
#pragma unroll
    for (int t = 0; t < 4; ++t)
#pragma unroll
      for (int i = 0; i < 5; ++i) xv[t][i] = ((const unsigned*)(x + (size_t)(s0 + t) * D_))[lane + 64 * i];
#pragma unroll
    for (int i = 0; i < 5; ++i) gwv[i] = ((const unsigned*)gw)[lane + 64 * i];
    float sum[4], gs[4], vs[4];
#pragma unroll
    for (int t = 0; t < 4; ++t) {
      sum[t] = 0.f; gs[t] = 0.f;
#pragma unroll
      for (int i = 0; i < 5; ++i) {
        sum[t] += bf_lo(xv[t][i]) + bf_hi(xv[t][i]);
        gs[t] += bf_lo(xv[t][i]) * bf_lo(gwv[i]) + bf_hi(xv[t][i]) * bf_hi(gwv[i]);
      }
    }
#pragma unroll
    for (int o = 32; o > 0; o >>= 1)
#pragma unroll
      for (int t = 0; t < 4; ++t) { sum[t] += __shfl_xor(sum[t], o); gs[t] += __shfl_xor(gs[t], o); }
#pragma unroll
    for (int t = 0; t < 4; ++t) {
      sum[t] *= (1.f / D_); vs[t] = 0.f;
#pragma unroll
      for (int i = 0; i < 5; ++i) {
        const float a = bf_lo(xv[t][i]) - sum[t], c = bf_hi(xv[t][i]) - sum[t];
        vs[t] += a * a + c * c;
      }
    }
#pragma unroll
    for (int o = 32; o > 0; o >>= 1)
#pragma unroll
      for (int t = 0; t < 4; ++t) vs[t] += __shfl_xor(vs[t], o);
#pragma unroll
    for (int t = 0; t < 4; ++t) {
      const float rstd = 1.f / sqrtf(vs[t] * (1.f / D_) + 1e-5f);
      unsigned* hr = (unsigned*)(ws + WS_H + (size_t)(s0 + t) * D_ * 2);
#pragma unroll
      for (int i = 0; i < 5; ++i)
        hr[lane + 64 * i] = pk_bf16((bf_lo(xv[t][i]) - sum[t]) * rstd, (bf_hi(xv[t][i]) - sum[t]) * rstd);
      if (lane == 0) ((float*)(ws + WS_G))[s0 + t] = 1.f / (1.f + __expf(-gs[t]));
    }
  }
}

constexpr int L2_LDS = 4 * T80_STG;

DI float gelu_tanh(float u) {
  const float z = 0.7978845608028654f * (u + 0.044715f * u * u * u);
  const float e = __expf(2.f * z);
  const float t = 1.f - 2.f / (e + 1.f);
  return 0.5f * u * (1.f + t);
}

DI void attn_stage(char* lds, const char* kv, int chunk, int buf, int wid, int lane) {
  const char* g = kv + (size_t)chunk * KV_CHUNK;
  char* l = lds + buf * KV_CHUNK;
#pragma unroll
  for (int r = 0; r < 3; ++r) {
    const int p = r * 8 + wid;
    if (p < 23) glds16(g + p * 1024, (unsigned)lane * 16u, l + p * 1024);
  }
}

typedef __attribute__((address_space(1))) unsigned gu32;

__global__ __launch_bounds__(512, 4) void k_main(const u16* __restrict__ w1, const u16* __restrict__ b1,
                                                 const u16* __restrict__ w2, const u16* __restrict__ b2,
                                                 const u16* __restrict__ wo, char* __restrict__ ws,
                                                 u16* __restrict__ out) {
  extern __shared__ __attribute__((aligned(16))) char lds[];
  const int tid = threadIdx.x, lane = tid & 63, wid = __builtin_amdgcn_readfirstlane(tid >> 6);
  const int bid = blockIdx.x ^ 256;
  if (bid < 256) {
    __builtin_amdgcn_s_setprio(3);
    const int xcd = bid & 7, i = bid >> 3;
    gu32* cnt = (gu32*)(ws + WS_CNT);
    __builtin_amdgcn_s_sleep(25);
    if (wid < 4) {
      u32x4 v[7];
#pragma unroll
      for (int r = 0; r < 6; ++r) v[r] = ((const u32x4*)w2)[bid * 1536 + r * 256 + tid];
      v[6] = ((const u32x4*)wo)[bid * 200 + (tid < 200 ? tid : 0)];
#pragma unroll
      for (int r = 0; r < 7; ++r) asm volatile("" :: "v"(v[r]));
    }
    if (i < 30) {
      const int mt = i / 3, nt = xcd * 3 + i % 3;
      const u16* A = (const u16*)(ws + WS_H) + mt * 64;
      const u16* B = w1 + nt * 64;
      const int wm = (wid >> 1) & 1, wn = wid & 1;
      f32x16 acc = {};
      constexpr int NBUF = 4, NK = 32;
      if (wid >= 4) {
        Stager<1, 1, 8, 8, false> st;
        st.init(A, D_, B, F_, wid - 4, lane);
        st.run<NBUF, 16384>(lds, NK);
      } else {
        for (int kt = 0; kt < NK; ++kt) {
          raw_barrier();
          const char* la = lds + (kt & 3) * 16384;
          bf16x8 af[4], bfr[4];
#pragma unroll
          for (int ks = 0; ks < 4; ++ks) { af[ks] = frag32_ks64(la, wm * 32, ks, lane); bfr[ks] = frag32_ks64(la + 8192, wn * 32, ks, lane); }
#pragma unroll
          for (int ks = 0; ks < 4; ++ks) {
            acc = mfma32(af[ks], bfr[ks], acc);
          }
        }
      }
      raw_barrier();
      u16* t = (u16*)(lds + 16384);
      if (wid < 4) {
        const int h = lane >> 5, fl = wn * 32 + (lane & 31);
        const float bb = bf2f(b1[nt * 64 + fl]);
#pragma unroll
        for (int r = 0; r < 16; ++r) t[(wm * 32 + crow(r, h)) * 64 + fl] = f2bf(gelu_tanh(acc[r] + bb));
      }
      __syncthreads();
      {
        const int row = tid >> 3, ch = tid & 7;
        const u32x4 v = *(const u32x4*)(lds + 16384 + tid * 16);
        u16* dst = (u16*)(ws + WS_U) + (size_t)(mt * 64 + row) * F_ + nt * 64 + ch * 8;
        asm volatile("global_store_dwordx4 %0, %1, off sc1\n\ts_nop 1" :: "v"(dst), "v"(v) : "memory");
      }
      wait_vm<0>();
      __syncthreads();
      if (tid == 0) __hip_atomic_fetch_add(cnt + mt * 32, 1u, __ATOMIC_RELAXED, __HIP_MEMORY_SCOPE_AGENT);
    }
    if (wid == 0) {
      for (int spin = 0; spin < (1 << 22); ++spin) {
        const unsigned c = __hip_atomic_load(cnt + (lane < 10 ? lane : 0) * 32, __ATOMIC_RELAXED, __HIP_MEMORY_SCOPE_AGENT);
        if (__all(c >= 24u)) break;
        __builtin_amdgcn_s_sleep(2);
      }
    }
    __syncthreads();
    {
      const int mt = xcd * 4 + (i >> 3), nt = i & 7;
      f32x4 acc[5] = {};
      gemm80<false, true, 4, true>(acc, lds, w2 + mt * 64, S_, (const u16*)(ws + WS_U) + (size_t)nt * 80 * F_, F_, 24, tid);
      if (wid == 0) {
        for (int spin = 0; spin < (1 << 22); ++spin) {
          const unsigned c = __hip_atomic_load(cnt + (16 + mt) * 32, __ATOMIC_RELAXED, __HIP_MEMORY_SCOPE_AGENT);
          if (c >= 8u) break;
          __builtin_amdgcn_s_sleep(4);
        }
      }
      float gv[4], bv[4];
      if (wid < 4) {
#pragma unroll
        for (int r = 0; r < 4; ++r) {
          const int row = mt * 64 + wid * 16 + (lane >> 4) * 4 + r;
          gv[r] = ((const float*)(ws + WS_G))[row]; bv[r] = bf2f(b2[row]);
        }
      }
      __syncthreads();
      f32x4 acca[5] = {};
      gemm80<true, false, 4, true>(acca, lds, (const u16*)(ws + WS_O) + (size_t)mt * 64 * D_, D_, wo + nt * 80, D_, 10, tid);
      u16* t = (u16*)lds;
      if (wid < 4) {
#pragma unroll
        for (int r = 0; r < 4; ++r) {
          const int row = wid * 16 + (lane >> 4) * 4 + r;
#pragma unroll
          for (int j = 0; j < 5; ++j)
            t[row * 80 + j * 16 + (lane & 15)] = f2bf(gv[r] * acca[j][r] + (1.f - gv[r]) * (acc[j][r] + bv[r]));
        }
      }
      __syncthreads();
      u16* o = out + (size_t)mt * 64 * D_ + nt * 80;
      for (int c = tid; c < 640; c += 512) {
        const int row = c / 10, ch = c % 10;
        *(u32x4*)(o + (size_t)row * D_ + ch * 8) = *(const u32x4*)(lds + c * 16);
      }
    }
    return;
  }
  const int ab = bid - 256;
  const int s = ((ab & 7) * 32 + (ab >> 3)) * 8 + wid;
  const int h = lane >> 5, hd = lane & 31;
  const char* kv = ws + WS_KV;
  float* red = (float*)(lds + 3 * KV_CHUNK);
  if (tid == 0) red[0] = 0.f;
  const u16* qr = (const u16*)(ws + WS_Q) + (size_t)s * D_ + hd * 20;
  const u32x2 qa = *(const u32x2*)(qr + 4 * h), qb = *(const u32x2*)(qr + 8 + 4 * h);
  const u32x2 qc = *(const u32x2*)(qr + 16);
  float kmax2 = ((const float*)(ws + WS_KN))[hd];
  attn_stage(lds, kv, 0, 0, wid, lane);
  attn_stage(lds, kv, 1, 1, wid, lane);
#pragma unroll
  for (int o = 16; o > 0; o >>= 1) kmax2 = fmaxf(kmax2, __shfl_xor(kmax2, o));
  float qn2 = 0.f;
#pragma unroll
  for (int i = 0; i < 2; ++i) {
    qn2 += bf_lo(qa[i]) * bf_lo(qa[i]) + bf_hi(qa[i]) * bf_hi(qa[i]);
    qn2 += bf_lo(qb[i]) * bf_lo(qb[i]) + bf_hi(qb[i]) * bf_hi(qb[i]);
    if (h == 0) qn2 += bf_lo(qc[i]) * bf_lo(qc[i]) + bf_hi(qc[i]) * bf_hi(qc[i]);
  }
  qn2 += __shfl_xor(qn2, 32);
  u32x4 b1u; b1u[0] = qa[0]; b1u[1] = qa[1]; b1u[2] = qb[0]; b1u[3] = qb[1];
  const bf16x8 B1 = __builtin_bit_cast(bf16x8, b1u);
  float mshift = sqrtf(qn2 * kmax2);
  const int ka_lane = lane * 16, kb_lane = KV_KB_OFF + lane * 8;
  const int v_lane = KV_V_OFF + h * 352 + (hd < 21 ? hd : 21) * 16;
  f32x16 O;
  for (int pass = 0; pass < 2; ++pass) {
    if (pass == 1) {
      s16x4 B2z; { u32x2 t = qc; if (h) { t[0] = 0; t[1] = 0; } B2z = __builtin_bit_cast(s16x4, t); }
      float mx = -3.0e38f;
      for (int c = 0; c < 8; ++c) {
        attn_stage(lds, kv, c, 0, wid, lane);
        wait_vm<0>(); raw_barrier();
        for (int t = 0; t < 8; ++t) {
          const bf16x8 A1 = *(const bf16x8*)(lds + t * 1024 + ka_lane);
          const s16x4 A2 = *(const s16x4*)(lds + t * 512 + kb_lane);
          f32x16 Sx = {};
          Sx = mfma32(A1, B1, Sx); Sx = mfma32k8(A2, B2z, Sx);
#pragma unroll
          for (int r = 0; r < 16; ++r) mx = fmaxf(mx, Sx[r]);
        }
        raw_barrier();
      }
      mx = fmaxf(mx, __shfl_xor(mx, 32));
      mshift = mx;
      attn_stage(lds, kv, 0, 0, wid, lane);
      attn_stage(lds, kv, 1, 1, wid, lane);
    }
    s16x4 B2;
    {
      const float m0 = bf2f(f2bf(mshift)), m1 = bf2f(f2bf(mshift - m0)), m2 = bf2f(f2bf(mshift - m0 - m1));
      u32x2 t = qc;
      if (h) { t[0] = pk_bf16(-m0, -m1); t[1] = pk_bf16(-m2, 0.f); }
      B2 = __builtin_bit_cast(s16x4, t);
    }
    O = f32x16{};
    f32x16 Sa, Sb;
    bf16x8 KA1; s16x4 KA2;
    auto tile = [&](const f32x16& Sc, f32x16& Sn, const char* vp, const char* k2a, const char* k2b) {
      const bf16x8 V0 = *(const bf16x8*)(vp), V1 = *(const bf16x8*)(vp + 704);
      u32x4 p0, p1;
      p0[0] = pk_bf16(__builtin_amdgcn_exp2f(Sc[0]), __builtin_amdgcn_exp2f(Sc[1]));
      p0[1] = pk_bf16(__builtin_amdgcn_exp2f(Sc[2]), __builtin_amdgcn_exp2f(Sc[3]));
      SB();
      Sn = mfma32(KA1, B1, f32x16{});
      SB();
      p0[2] = pk_bf16(__builtin_amdgcn_exp2f(Sc[4]), __builtin_amdgcn_exp2f(Sc[5]));
      p0[3] = pk_bf16(__builtin_amdgcn_exp2f(Sc[6]), __builtin_amdgcn_exp2f(Sc[7]));
      SB();
      Sn = mfma32k8(KA2, B2, Sn);
      KA1 = *(const bf16x8*)(k2a); KA2 = *(const s16x4*)(k2b);
      SB();
      p1[0] = pk_bf16(__builtin_amdgcn_exp2f(Sc[8]), __builtin_amdgcn_exp2f(Sc[9]));
      p1[1] = pk_bf16(__builtin_amdgcn_exp2f(Sc[10]), __builtin_amdgcn_exp2f(Sc[11]));
      SB();
      O = mfma32(V0, __builtin_bit_cast(bf16x8, p0), O);
      SB();
      p1[2] = pk_bf16(__builtin_amdgcn_exp2f(Sc[12]), __builtin_amdgcn_exp2f(Sc[13]));
      p1[3] = pk_bf16(__builtin_amdgcn_exp2f(Sc[14]), __builtin_amdgcn_exp2f(Sc[15]));
      SB();
      O = mfma32(V1, __builtin_bit_cast(bf16x8, p1), O);
      SB();
    };
    int bc = 0;
    for (int c = 0; c < 8; ++c) {
      wait_vm<0>(); raw_barrier();
      int bn = bc + 1; if (bn == 3) bn = 0;
      int b2n = bn + 1; if (b2n == 3) b2n = 0;
      if (c + 2 < 8) attn_stage(lds, kv, c + 2, b2n, wid, lane);
      const char* l = lds + bc * KV_CHUNK;
      const char* ln = lds + bn * KV_CHUNK;
      if (c == 0) {
        const bf16x8 A1 = *(const bf16x8*)(l + ka_lane);
        const s16x4 A2 = *(const s16x4*)(l + kb_lane);
        KA1 = *(const bf16x8*)(l + 1024 + ka_lane); KA2 = *(const s16x4*)(l + 512 + kb_lane);
        Sa = mfma32(A1, B1, f32x16{}); Sa = mfma32k8(A2, B2, Sa);
      }
#pragma unroll
      for (int t = 0; t < 8; t += 2) {
        const char* ka = t + 2 < 8 ? l + (t + 2) * 1024 : ln + (t - 6) * 1024;
        const char* kb = t + 2 < 8 ? l + (t + 2) * 512 : ln + (t - 6) * 512;
        tile(Sa, Sb, l + v_lane + t * 1408, ka + ka_lane, kb + kb_lane);
        tile(Sb, Sa, l + v_lane + (t + 1) * 1408, ka + 1024 + ka_lane, kb + 512 + kb_lane);
      }
      bc = bn;
    }
    const float lsum = __shfl(O[8], hd + 32);
    const bool bad = !(lsum >= 8.271806e-25f && lsum <= 3.0e38f);
    if (pass == 1) break;
    if (__any(bad) && lane == 0) red[0] = 1.f;
    __syncthreads();
    if (red[0] == 0.f) break;
  }
  {
    const float lsum = __shfl(O[8], hd + 32);
    const float inv = 1.f / lsum;
    u16* orow = (u16*)(ws + WS_O) + (size_t)s * D_ + hd * 20 + 4 * h;
    u32x2 a, b;
    a[0] = pk_bf16(O[0] * inv, O[1] * inv); a[1] = pk_bf16(O[2] * inv, O[3] * inv);
    b[0] = pk_bf16(O[4] * inv, O[5] * inv); b[1] = pk_bf16(O[6] * inv, O[7] * inv);
    typedef __attribute__((address_space(1))) unsigned long long gu64;
    __hip_atomic_store((gu64*)(orow), __builtin_bit_cast(unsigned long long, a), __ATOMIC_RELAXED, __HIP_MEMORY_SCOPE_AGENT);
    __hip_atomic_store((gu64*)(orow + 8), __builtin_bit_cast(unsigned long long, b), __ATOMIC_RELAXED, __HIP_MEMORY_SCOPE_AGENT);
    if (h == 0) {
      u32x2 c; c[0] = pk_bf16(O[8] * inv, O[9] * inv); c[1] = pk_bf16(O[10] * inv, O[11] * inv);
      __hip_atomic_store((gu64*)(orow + 16), __builtin_bit_cast(unsigned long long, c), __ATOMIC_RELAXED, __HIP_MEMORY_SCOPE_AGENT);
    }
    wait_vm<0>();
    __syncthreads();
    if (tid == 0) __hip_atomic_fetch_add((gu32*)(ws + WS_CNT) + (16 + (s >> 6)) * 32, 1u, __ATOMIC_RELAXED, __HIP_MEMORY_SCOPE_AGENT);
  }
}

extern "C" void kernel_launch(void* const* d_in, const int* in_sizes, int n_in, void* d_out, int out_size,
                              void* d_ws, size_t ws_size, hipStream_t stream) {
  const u16* x = (const u16*)d_in[0];
  const u16* wq = (const u16*)d_in[1];
  const u16* wk = (const u16*)d_in[2];
  const u16* wv = (const u16*)d_in[3];
  const u16* wo = (const u16*)d_in[4];
  const u16* w1 = (const u16*)d_in[6];
  const u16* b1 = (const u16*)d_in[7];
  const u16* w2 = (const u16*)d_in[8];
  const u16* b2 = (const u16*)d_in[9];
  const u16* gw = (const u16*)d_in[10];
  char* ws = (char*)d_ws;
  (void)hipFuncSetAttribute((const void*)k_proj, hipFuncAttributeMaxDynamicSharedMemorySize, L1_LDS);
  (void)hipFuncSetAttribute((const void*)k_main, hipFuncAttributeMaxDynamicSharedMemorySize, L2_LDS);
  k_proj<<<L1_NQ + L1_NKV + L1_NLN + L1_NPF, 512, L1_LDS, stream>>>(x, wq, wk, wv, gw, w1, ws);
  k_main<<<512, 512, L2_LDS, stream>>>(w1, b1, w2, b2, wo, ws, (u16*)d_out);
}
```
